```python
import torch

def _softmax(x, axis=-1):
    return torch.softmax(x, dim=axis)
def _gelu(x):
    return torch.nn.functional.gelu(x, approximate="tanh")
def _silu(x):
    return torch.nn.functional.silu(x)
def _sigmoid(x):
    return torch.sigmoid(x)
def _layernorm(x, axis=-1, eps=1e-5):
    m = x.mean(dim=axis, keepdim=True)
    v = x.var(dim=axis, keepdim=True, unbiased=False)
    return (x - m) / torch.sqrt(v + eps)
def _scatter_add(tgt, idx, src, axis=0):
    return tgt.clone().index_add_(axis, idx, src)
def _segment_sum(src, seg, n):
    out = torch.zeros((n,) + src.shape[1:], dtype=src.dtype)
    return out.index_add_(0, seg, src)
def _pad(x, pw):
    flat = tuple(v for pair in reversed(pw) for v in pair)
    return torch.nn.functional.pad(x, flat)

kernel_name = 'parallel_sum__ssm_conv_h3__glu_gated_geglu__prompt'

def setup_inputs():

    _w = (lambda a: (lambda t: t.to(torch.bfloat16) if t.is_floating_point() else t)(torch.as_tensor(a)))
    def _A_setup():
        B, S, D = 1, 2048, 640
        ks = 4
        return {
            "q": _w((lambda *s: torch.randn(*s, dtype=torch.bfloat16) * 0.05)(B, S, D)),
            "k": _w((lambda *s: torch.randn(*s, dtype=torch.bfloat16) * 0.2)(B, S, D)),
            "v": _w((lambda *s: torch.randn(*s, dtype=torch.bfloat16) * 0.2)(B, S, D)),
            "shift_k": _w((lambda *s: torch.randn(*s, dtype=torch.bfloat16) * 0.2)(D, ks) * 0.5),
            "ssm_k": _w((lambda *s: torch.randn(*s, dtype=torch.bfloat16) * 0.4)(D, S) * (1.0 / S ** 0.5)),
            "ssm_D": _w((lambda *s: torch.randn(*s, dtype=torch.bfloat16) * 0.2)(D)),
        }
    _A_in = _A_setup()

    def _B_setup():
        B, S, Dm, Df = 1, 2048, 640, 1728
        return {
            "x": _w((lambda *s: torch.randn(*s, dtype=torch.bfloat16) * 0.05)(B, S, Dm)),
            "w_gate": _w((lambda *s: torch.randn(*s, dtype=torch.bfloat16) * 0.4)(Dm, Df) * (Dm ** -0.5)),
            "w_up": _w((lambda *s: torch.randn(*s, dtype=torch.bfloat16) * 0.4)(Dm, Df) * (Dm ** -0.5)),
            "w_down": _w((lambda *s: torch.randn(*s, dtype=torch.bfloat16) * 0.4)(Df, Dm) * (Df ** -0.5)),
        }
    _B_in = _B_setup()

    return {
        "A_q": _A_in["q"],
        "A_k": _A_in["k"],
        "A_v": _A_in["v"],
        "A_shift_k": _A_in["shift_k"],
        "A_ssm_k": _A_in["ssm_k"],
        "A_ssm_D": _A_in["ssm_D"],
        "B_x": _B_in["x"],
        "B_w_gate": _B_in["w_gate"],
        "B_w_up": _B_in["w_up"],
        "B_w_down": _B_in["w_down"],
    }

def reference(A_q, A_k, A_v, A_shift_k, A_ssm_k, A_ssm_D, B_x, B_w_gate, B_w_up, B_w_down):
    def _A_ref(q, k, v, shift_k, ssm_k, ssm_D):
        B, S, D = q.shape
        ks = shift_k.shape[-1]
        k_pad = _pad(k, ((0, 0), (ks - 1, 0), (0, 0)))
        k_shift = torch.zeros_like(k)
        for t in range(ks):
            k_shift = k_shift + k_pad[:, t:t + S, :] * shift_k[:, ks - 1 - t][None, None, :]
        kv = k_shift * v
        L = 2 * S
        kv_pad = _pad(kv, ((0, 0), (0, S), (0, 0)))
        kf_pad = _pad(ssm_k, ((0, 0), (0, S)))
        kv_f = (lambda x, axes=(-2,-1): torch.fft.rfft2(x.float(), dim=axes))(kv_pad[:, None, :, :], axes=(1, 2))
        kf_f = (lambda x, axes=(-2,-1): torch.fft.rfft2(x.float(), dim=axes))(kf_pad[:, None, :], axes=(1, 2))
        kf_f = (lambda x, *d: x.permute(*d))(kf_f, 1, 2, 0)
        y_f = kv_f * kf_f[None, :, :, :]
        y = (lambda x, s, axes=(-2,-1): torch.fft.irfft2(x, s=s, dim=axes).to(torch.bfloat16))(y_f, (1, L), axes=(1, 2))[:, 0, :S, :]
        y = y + kv * ssm_D[None, None, :]
        return q * y

    def _B_ref(x, w_gate, w_up, w_down):
        g = torch.einsum("bsd,df->bsf", x, w_gate)
        u = torch.einsum("bsd,df->bsf", x, w_up)
        h = _gelu(g) * u
        return torch.einsum("bsf,fd->bsd", h, w_down)

    x = A_q
    xn = _layernorm(x, axis=-1)
    return x + _A_ref(xn, A_k, A_v, A_shift_k, A_ssm_k, A_ssm_D) + _B_ref(xn, B_w_gate, B_w_up, B_w_down)
```

```cpp
#include <hip/hip_runtime.h>
#include <hip/hip_bf16.h>
typedef unsigned short u16;
__device__ __forceinline__ float bf2f(u16 x){ return __uint_as_float(((unsigned)x)<<16); }
__device__ __forceinline__ u16 f2bf(float f){ __hip_bfloat16 b = __float2bfloat16(f); return *reinterpret_cast<u16*>(&b); }
constexpr int S=2048, D=640, F=1728;

__global__ void k_ln(const u16* x, u16* xn){
  int t=blockIdx.x; int lane=threadIdx.x;
  float s=0; for(int i=lane;i<D;i+=64) s+=bf2f(x[t*D+i]);
  for(int o=32;o;o>>=1) s+=__shfl_xor(s,o);
  float m=s/D; float v=0; for(int i=lane;i<D;i+=64){float d=bf2f(x[t*D+i])-m; v+=d*d;}
  for(int o=32;o;o>>=1) v+=__shfl_xor(v,o);
  v/=D; float r=rsqrtf(v+1e-5f);
  for(int i=lane;i<D;i+=64) xn[t*D+i]=f2bf((bf2f(x[t*D+i])-m)*r);
}
__global__ void k_kv(const u16* k,const u16* v,const u16* sk,u16* kv){
  int i=blockIdx.x*256+threadIdx.x; int t=i/D,d=i%D;
  float a=0; for(int j=0;j<4;++j){ int tt=t-j; if(tt>=0) a+=bf2f(k[tt*D+d])*bf2f(sk[d*4+j]); }
  kv[i]=f2bf(a*bf2f(v[i]));
}
__global__ void k_conv(const u16* kv,const u16* sk,const u16* Dp,float* y){
  int i=blockIdx.x*256+threadIdx.x; int t=i/D,d=i%D;
  float a=0; for(int s=0;s<=t;++s) a+=bf2f(kv[s*D+d])*bf2f(sk[d*S+t-s]);
  y[i]=a+bf2f(kv[i])*bf2f(Dp[d]);
}
__global__ void k_g1(const u16* xn,const u16* wg,const u16* wu,u16* h){
  int i=blockIdx.x*256+threadIdx.x; int t=i/F,f=i%F;
  float g=0,u=0; for(int k=0;k<D;++k){ float a=bf2f(xn[t*D+k]); g+=a*bf2f(wg[k*F+f]); u+=a*bf2f(wu[k*F+f]); }
  float ge=0.5f*g*(1.f+tanhf(0.7978845608f*(g+0.044715f*g*g*g)));
  h[i]=f2bf(ge*u);
}
__global__ void k_g2(const u16* h,const u16* wd,const u16* x,const u16* xn,const float* y,u16* out){
  int i=blockIdx.x*256+threadIdx.x; int t=i/D,d=i%D;
  float a=0; for(int k=0;k<F;++k) a+=bf2f(h[t*F+k])*bf2f(wd[k*D+d]);
  out[i]=f2bf(bf2f(x[i])+bf2f(xn[i])*y[i]+a);
}
extern "C" void kernel_launch(void* const* d_in, const int* in_sizes, int n_in, void* d_out, int out_size, void* d_ws, size_t ws_size, hipStream_t stream){
  const u16* q=(const u16*)d_in[0]; const u16* k=(const u16*)d_in[1]; const u16* v=(const u16*)d_in[2];
  const u16* shk=(const u16*)d_in[3]; const u16* ssk=(const u16*)d_in[4]; const u16* ssD=(const u16*)d_in[5];
  const u16* wg=(const u16*)d_in[7]; const u16* wu=(const u16*)d_in[8]; const u16* wd=(const u16*)d_in[9];
  char* ws=(char*)d_ws;
  u16* xn=(u16*)ws; u16* kv=(u16*)(ws+(4<<20)); float* y=(float*)(ws+(8<<20)); u16* h=(u16*)(ws+(16<<20));
  k_ln<<<S,64,0,stream>>>(q,xn);
  k_kv<<<S*D/256,256,0,stream>>>(k,v,shk,kv);
  k_conv<<<S*D/256,256,0,stream>>>(kv,ssk,ssD,y);
  k_g1<<<S*F/256,256,0,stream>>>(xn,wg,wu,h);
  k_g2<<<S*D/256,256,0,stream>>>(h,wd,q,xn,y,(u16*)d_out);
}
```

```cpp
#include <hip/hip_runtime.h>
#include <hip/hip_bf16.h>

typedef unsigned short u16;
typedef unsigned int u32;
typedef unsigned long long u64;
typedef __attribute__((ext_vector_type(8))) __bf16 bf16x8;
typedef __attribute__((ext_vector_type(2))) __bf16 bf16x2;
typedef __attribute__((ext_vector_type(4))) short s16x4;
typedef __attribute__((ext_vector_type(4))) float f32x4;
typedef __attribute__((ext_vector_type(2))) float f32x2;
typedef __attribute__((ext_vector_type(4))) u32 u32x4;
typedef __attribute__((ext_vector_type(2))) u32 u32x2;
typedef __amdgpu_buffer_rsrc_t rsrc_t;
#define GAS __attribute__((address_space(1)))
#define LAS __attribute__((address_space(3)))
#define DI __device__ __forceinline__
#define BAR() __builtin_amdgcn_s_barrier()
#define WAITV(n) asm volatile("s_waitcnt vmcnt(" #n ")" ::: "memory")
#define WAITL(n) asm volatile("s_waitcnt lgkmcnt(" #n ")" ::: "memory")
#define SCHED() __builtin_amdgcn_sched_barrier(0)
#define RLX_AGENT __ATOMIC_RELAXED, __HIP_MEMORY_SCOPE_AGENT

constexpr int S = 2048, D = 640, F = 1728;
constexpr u32 OFF_HT = 0;
constexpr u32 OFF_WDT = 8u << 20;
constexpr u32 OFF_UP = 11u << 20;
constexpr u32 OFF_Z = 14u << 20;
constexpr u32 OFF_ST = 17u << 20;
constexpr u32 OFF_FL = (17u << 20) + 65536;
constexpr u32 FL_EPOCH = OFF_FL;
constexpr u32 FL_ACK = OFF_FL + 1024;
constexpr u32 FL_AUX = OFF_FL + 2048;
constexpr u32 FL_H = OFF_FL + 4096;
constexpr u32 FL_Z = OFF_FL + 8192;
constexpr u32 WS_USED = OFF_FL + 16384;
constexpr int SPIN_MAX = 1 << 18;


DI float bflo(u32 v) { return __uint_as_float(v << 16); }
DI float bfhi(u32 v) { return __uint_as_float(v & 0xffff0000u); }
DI u32 pk2(float a, float b) { f32x2 v = {a, b}; bf16x2 r = __builtin_convertvector(v, bf16x2); return __builtin_bit_cast(u32, r); }
DI u32 pk2v(f32x2 v) { bf16x2 r = __builtin_convertvector(v, bf16x2); return __builtin_bit_cast(u32, r); }
DI f32x4 mfma(bf16x8 a, bf16x8 b, f32x4 c) { return __builtin_amdgcn_mfma_f32_16x16x32_bf16(a, b, c, 0, 0, 0); }
DI u32x2 trd(LAS char* p) { s16x4 v = __builtin_amdgcn_ds_read_tr16_b64_v4i16((LAS s16x4*)p); return __builtin_bit_cast(u32x2, v); }
DI bf16x8 cat(u32x2 a, u32x2 b) { u32x4 v = {a.x, a.y, b.x, b.y}; return __builtin_bit_cast(bf16x8, v); }
DI void glds(const char* g, LAS char* l) { __builtin_amdgcn_global_load_lds((const GAS u32*)g, (LAS u32*)l, 16, 0, 0); }
DI float dot2(u32 a, u32 b, float c) { return __builtin_amdgcn_fdot2_f32_bf16(__builtin_bit_cast(bf16x2, a), __builtin_bit_cast(bf16x2, b), c, false); }
DI rsrc_t mkrsrc(const void* p, u32 n) { return __builtin_amdgcn_make_buffer_rsrc((void*)p, 0, n, 0x00020000); }
DI u32x4 ld_sc1(rsrc_t r, u32 vo, u32 so) { return __builtin_amdgcn_raw_buffer_load_b128(r, vo, so, 16); }
DI void st_sc1(rsrc_t r, u32x4 v, u32 vo, u32 so) { __builtin_amdgcn_raw_buffer_store_b128(v, r, vo, so, 16); }
DI u32 ldflag(char* ws, u32 off) { return __hip_atomic_load((GAS u32*)(ws + off), RLX_AGENT); }
DI void stflag(char* ws, u32 off, u32 v) { __hip_atomic_store((GAS u32*)(ws + off), v, RLX_AGENT); }
DI void wait_flags(char* ws, u32 off, bool act, u32 T) {
  for (int spins = 0; spins < SPIN_MAX; ++spins) {
    const u32 v = ldflag(ws, off);
    if (__all(!act || v == T)) break;
    __builtin_amdgcn_s_sleep(2);
  }
  asm volatile("" ::: "memory");
}

constexpr int STG = 49152;
constexpr int L_ST = 3 * STG;
constexpr int L_CS = L_ST + 4096;
constexpr int L_T = L_CS + 512;
constexpr int L_Z = L_T + 64;
constexpr int LDS_SZ = L_Z + 10240;
constexpr int CV_K = 64, CV_U = 4160, CVSZ = 4160 + 4 * 1152;
constexpr int L_CV = STG;
constexpr int L_RED = 2 * STG;
constexpr int WPRE = 4;

DI void conv_channel(LAS char* cb, char* ws, const u16* ssk, const u16* ssD, int ch, int lane, u32 T, int wid) {
  const int g = lane >> 4, q = (lane >> 2) & 3, p = lane & 3, tl = lane & 15;
  const rsrc_t rws = mkrsrc(ws, WS_USED);
  u32x4 kin[4], uin[4];
#pragma unroll
  for (int i = 0; i < 4; ++i) uin[i] = ld_sc1(rws, lane * 16, OFF_UP + ch * 4096 + i * 1024);
#pragma unroll
  for (int i = 0; i < 4; ++i) kin[i] = *(const GAS u32x4*)((const char*)ssk + (size_t)ch * 4096 + i * 1024 + lane * 16);
  const float Dd = bflo(ssD[ch]);
  if (lane < 16) *(LAS u32*)(cb + lane * 4) = 0u;
  *(LAS u32x2*)(cb + CV_U + g * 1152 + tl * 8) = u32x2{0u, 0u};
#pragma unroll
  for (int i = 0; i < 4; ++i) *(LAS u32x4*)(cb + CV_U + i * 1152 + 128 + lane * 16) = uin[i];
#pragma unroll
  for (int i = 0; i < 4; ++i) *(LAS u32x4*)(cb + CV_K + i * 1024 + lane * 16) = kin[i];
  bf16x8 A[16];
  {
    LAS char* a = cb + CV_K + (32 * g + 16 - 4 * q + 4 * p) * 2;
#pragma unroll
    for (int s = 0; s < 16; ++s) A[s] = cat(trd(a + s * 256), trd(a + s * 256 - 32));
  }
  f32x4 acc[4][8];
#pragma unroll
  for (int r = 0; r < 4; ++r)
#pragma unroll
    for (int n = 0; n < 8; ++n) acc[r][n] = f32x4{0.f, 0.f, 0.f, 0.f};
  LAS char* ubase = cb + CV_U + 128 + (4 * tl - 8 * g - 4) * 2;
  bf16x8 B[2][4];
  auto ldb = [&](int e) {
#pragma unroll
    for (int r = 0; r < 4; ++r) { LAS char* a = ubase + r * 1152 + e * 64; B[e & 1][r] = cat(*(LAS u32x2*)a, *(LAS u32x2*)(a + 8)); }
  };
  ldb(-1);
  SCHED();
#pragma unroll
  for (int e = -1; e < 15; ++e) {
    if (e < 14) ldb(e + 1);
    SCHED();
#pragma unroll
    for (int r = 0; r < 4; ++r)
#pragma unroll
      for (int n = (e + 1) / 2; n < 8; ++n) acc[r][n] = mfma(A[2 * n - e], B[e & 1][r], acc[r][n]);
    SCHED();
  }
  const int lam = 4 * tl + g;
  const int sl = (lam + 63) & 63;
  const int srcl = (16 * (sl & 3) + (sl >> 2)) * 4;
  float pv0 = 0.f, pv1 = 0.f, pv2 = 0.f;
  GAS u64* zd = (GAS u64*)(ws + OFF_Z + (size_t)ch * 4096 + lam * 8);
#pragma unroll
  for (int n = 0; n < 8; ++n) {
    const f32x4 d0 = acc[0][n], d1 = acc[1][n], d2 = acc[2][n], d3 = acc[3][n];
    float y0 = d0[0], y1 = d0[1] + d1[0], y2 = d0[2] + d1[1] + d2[0], y3 = d0[3] + d1[2] + d2[1] + d3[0];
    const float cc0 = d1[3] + d2[2] + d3[1], cc1 = d2[3] + d3[2], cc2 = d3[3];
    const float i0 = __int_as_float(__builtin_amdgcn_ds_bpermute(srcl, __float_as_int(cc0)));
    const float i1 = __int_as_float(__builtin_amdgcn_ds_bpermute(srcl, __float_as_int(cc1)));
    const float i2 = __int_as_float(__builtin_amdgcn_ds_bpermute(srcl, __float_as_int(cc2)));
    y0 += lam == 0 ? pv0 : i0; y1 += lam == 0 ? pv1 : i1; y2 += lam == 0 ? pv2 : i2;
    pv0 = i0; pv1 = i1; pv2 = i2;
    LAS char* ua = cb + CV_U + 128 + (64 * n + lam) * 2;
    y0 = fmaf(bflo(*(LAS u16*)(ua)), Dd, y0);
    y1 = fmaf(bflo(*(LAS u16*)(ua + 1152)), Dd, y1);
    y2 = fmaf(bflo(*(LAS u16*)(ua + 2304)), Dd, y2);
    y3 = fmaf(bflo(*(LAS u16*)(ua + 3456)), Dd, y3);
    const u64 o = (u64)pk2(y0, y1) | ((u64)pk2(y2, y3) << 32);
    __hip_atomic_store(zd + n * 64, o, RLX_AGENT);
  }
  WAITV(0);
  if (lane == 0) stflag(ws, FL_Z + ((ch / 80) * 128 + ch % 80) * 4, T);
}

DI void aux_work(LAS char* lds, char* ws, const u16* x, const u16* kk, const u16* vv, const u16* shk, const u16* wd, int aux, int wid, int lane) {
  const int g = lane >> 4, q = (lane >> 2) & 3, p = lane & 3;
  const rsrc_t rws = mkrsrc(ws, WS_USED);
    const int c = lane & 7, rgp = lane >> 3;
    const int sw = aux * 8 + wid;
    u32x4 xr[10];
    if (sw < 256) {
#pragma unroll
      for (int i = 0; i < 10; ++i) xr[i] = *(const GAS u32x4*)((const char*)x + (size_t)(sw * 8 + rgp) * 1280 + i * 128 + c * 16);
    }
    const int unit = aux * 2 + (wid >> 2), tq = unit / 10, cq = unit % 10;
    const int w4 = wid & 3;
    const int t0 = tq * 256 + w4 * 64 + rgp * 8;
    u32x4 kr[11], vr[8], sk[4], wdv[2][4];
    const size_t cofs = (size_t)(cq * 64 + c * 8) * 2;
    const int id0 = aux * 8 + wid, id1 = id0 + 320;
    const int id1c = id1 < 540 ? id1 : id0;
#pragma unroll
    for (int r = 0; r < 2; ++r) {
      const int id = r ? id1c : id0, ks = id / 10, wq = id % 10;
#pragma unroll
      for (int i = 0; i < 4; ++i) wdv[r][i] = *(const GAS u32x4*)((const char*)wd + (size_t)(ks * 32 + i * 8 + rgp) * 1280 + wq * 128 + c * 16);
    }
#pragma unroll
    for (int i = 0; i < 11; ++i) {
      const int t = t0 - 3 + i;
      const int tc = t < 0 ? 0 : t;
      u32x4 v = *(const GAS u32x4*)((const char*)kk + (size_t)tc * 1280 + cofs);
      if (t < 0) v = u32x4{0, 0, 0, 0};
      kr[i] = v;
    }
#pragma unroll
    for (int i = 0; i < 8; ++i) vr[i] = *(const GAS u32x4*)((const char*)vv + (size_t)(t0 + i) * 1280 + cofs);
#pragma unroll
    for (int i = 0; i < 4; ++i) sk[i] = *(const GAS u32x4*)((const char*)shk + cofs * 4 + i * 16);
    SCHED();
    if (sw < 256) {
      float s1 = 0.f;
#pragma unroll
      for (int i = 0; i < 10; ++i)
#pragma unroll
        for (int j = 0; j < 4; ++j) s1 = dot2(xr[i][j], 0x3f803f80u, s1);
      s1 += __shfl_xor(s1, 1); s1 += __shfl_xor(s1, 2); s1 += __shfl_xor(s1, 4);
      const float mean = s1 * (1.f / D);
      float s2 = 0.f;
#pragma unroll
      for (int i = 0; i < 10; ++i)
#pragma unroll
        for (int j = 0; j < 4; ++j) { const float a = bflo(xr[i][j]) - mean, bb = bfhi(xr[i][j]) - mean; s2 = fmaf(a, a, s2); s2 = fmaf(bb, bb, s2); }
      s2 += __shfl_xor(s2, 1); s2 += __shfl_xor(s2, 2); s2 += __shfl_xor(s2, 4);
      const float var = s2 * (1.f / D) + 1e-5f;
      const float rstd = __builtin_amdgcn_rsqf(var);
      f32x4 st = {mean, rstd, var * rstd, 0.f};
      if (c == 0) st_sc1(rws, __builtin_bit_cast(u32x4, st), rgp * 16, OFF_ST + sw * 128);
    }
    LAS char* wl = lds + 65536 + wid * 4096;
#pragma unroll
    for (int r = 0; r < 2; ++r) {
      const int id = r ? id1c : id0, ks = id / 10, wq = id % 10;
      if (r == 0 || id1 < 540) {
#pragma unroll
        for (int i = 0; i < 4; ++i) { const int row = i * 8 + rgp; *(LAS u32x4*)(wl + row * 128 + ((c ^ (((row >> 1) & 3) << 1)) << 4)) = wdv[r][i]; }
        const int row = 4 * g + q;
#pragma unroll
        for (int dt = 0; dt < 4; ++dt) {
          LAS char* a = wl + row * 128 + (((dt * 2 + (p >> 1)) ^ (((row >> 1) & 3) << 1)) << 4) + (p & 1) * 8;
          u32x2 lo = trd(a), hi = trd(a + 2048);
          const int Dg = wq * 4 + dt, cc = Dg / 5, jj = Dg % 5;
          u32x4 o = {lo.x, lo.y, hi.x, hi.y};
          st_sc1(rws, o, lane * 16, OFF_WDT + ((cc * 54 + ks) * 5 + jj) * 1024);
        }
      }
    }
    LAS char* ub = lds + (wid >> 2) * 32768;
#pragma unroll
    for (int j = 0; j < 8; ++j) {
      float kf[11], tap[4];
#pragma unroll
      for (int i = 0; i < 11; ++i) kf[i] = (j & 1) ? bfhi(kr[i][j >> 1]) : bflo(kr[i][j >> 1]);
      tap[0] = bflo(sk[j >> 1][(j & 1) * 2]); tap[1] = bfhi(sk[j >> 1][(j & 1) * 2]);
      tap[2] = bflo(sk[j >> 1][(j & 1) * 2 + 1]); tap[3] = bfhi(sk[j >> 1][(j & 1) * 2 + 1]);
      float u[8];
#pragma unroll
      for (int i = 0; i < 8; ++i) {
        float a = kf[i] * tap[3]; a = fmaf(kf[i + 1], tap[2], a); a = fmaf(kf[i + 2], tap[1], a); a = fmaf(kf[i + 3], tap[0], a);
        u[i] = a * ((j & 1) ? bfhi(vr[i][j >> 1]) : bflo(vr[i][j >> 1]));
      }
#pragma unroll
      for (int r = 0; r < 4; ++r) *(LAS u32*)(ub + (c * 8 + j) * 512 + r * 128 + ((w4 * 8 + rgp + 4 * c) & 31) * 4) = pk2(u[r], u[r + 4]);
    }
    WAITL(0);
    BAR();
#pragma unroll
    for (int it = 0; it < 8; ++it) {
      const int line = w4 * 64 + it * 8 + rgp, ch = line >> 2, r = line & 3;
      u32x4 v = *(LAS u32x4*)(ub + line * 128 + (((c + (ch >> 3)) & 7) << 4));
      st_sc1(rws, v, ((ch * 4 + r) * 1024) + c * 16, OFF_UP + cq * 64 * 4096 + tq * 128);
    }
    WAITV(0);
    BAR();
    const u32 T = __builtin_amdgcn_readfirstlane(*(LAS u32*)(lds + L_T));
    if (wid == 0 && lane == 0) stflag(ws, FL_AUX + aux * 4, T);
    wait_flags(ws, FL_AUX + (lane < 40 ? lane : 0) * 4, lane < 40, T);
}

__global__ __launch_bounds__(512, 2) void kf(const u16* __restrict__ x, const u16* __restrict__ kk, const u16* __restrict__ vv,
                                             const u16* __restrict__ shk, const u16* __restrict__ ssk, const u16* __restrict__ ssD,
                                             const u16* __restrict__ wg, const u16* __restrict__ wu, const u16* __restrict__ wd,
                                             char* __restrict__ ws, u16* __restrict__ out) {
  __shared__ __attribute__((aligned(1024))) char lds_[LDS_SZ];
  LAS char* lds = (LAS char*)lds_;
  const int b = blockIdx.x, xcd = b & 7, slot = b >> 3;
  const int cg = xcd & 3, rg = xcd >> 2;
  const int ncol = cg == 3 ? 6 : 7;
  const int wid = __builtin_amdgcn_readfirstlane(threadIdx.x >> 6), lane = threadIdx.x & 63;
  const int g = lane >> 4, q = (lane >> 2) & 3, p = lane & 3, tl = lane & 15;
  const bool isg = slot < 4 * ncol;
  const int aux = cg < 3 ? (rg * 3 + cg) * 4 + (slot - 28) : 24 + rg * 8 + (slot - 24);
  const int rt = rg * 4 + (slot & 3), ct = cg * 7 + (slot >> 2);
  const int rq = (xcd >> 1) * 8 + (slot & 7), c2 = (xcd & 1) * 4 + (slot >> 3);
  const rsrc_t rws = mkrsrc(ws, WS_USED);
  if (wid == 0) {
    const u32 E = __builtin_amdgcn_readfirstlane(ldflag(ws, FL_EPOCH));
    const u32 T0 = E * 1664525u + 1013904223u;
    if (lane == 0) { *(LAS u32*)(lds + L_T) = T0; stflag(ws, FL_ACK + b * 4, T0); }
    WAITL(0);
  }

  if (wid < 4) {
  bf16x8 W2[WPRE][5];
  const int ks0 = wid < 2 ? wid * 14 : 28 + (wid - 2) * 13;
  const u32 wso = OFF_WDT + ((c2 * 54 + ks0) * 5) * 1024;
    if (isg) {
    const int wr = wid >> 1, wc = wid & 1;
    u32 ax0 = (wr * 128 + tl) * 128 + ((g ^ (tl >> 1)) << 4);
    u32 ax1 = ax0 ^ 64;
    const int f = ((q >> 1) & 1) | ((g & 1) << 1);
    u32 aw0 = 32768 + (8 * g + q) * 128 + (((wc * 4 + (p >> 1)) ^ (f << 1)) << 4) + (p & 1) * 8;
    u32 aw1 = aw0 ^ 32;
    f32x4 acc[8][4];
#pragma unroll
    for (int i = 0; i < 8; ++i)
#pragma unroll
      for (int j = 0; j < 4; ++j) acc[i][j] = f32x4{0.f, 0.f, 0.f, 0.f};
    {
      bf16x8 X0[8], W0[4], X1[8], W1[4];
      auto ldf = [&](bf16x8(&X)[8], bf16x8(&W)[4], u32 ax, u32 wa, u32 wb2, int ko) {
        W[0] = cat(trd(lds + wa + ko), trd(lds + wa + ko + 512));
        X[0] = *(LAS bf16x8*)(lds + ax);
        W[1] = cat(trd(lds + wb2 + ko), trd(lds + wb2 + ko + 512));
        W[2] = cat(trd(lds + wa + ko + 8192), trd(lds + wa + ko + 8192 + 512));
        W[3] = cat(trd(lds + wb2 + ko + 8192), trd(lds + wb2 + ko + 8192 + 512));
#pragma unroll
        for (int i = 1; i < 8; ++i) X[i] = *(LAS bf16x8*)(lds + ax + i * 2048);
      };
      auto mma = [&](bf16x8(&X)[8], bf16x8(&W)[4]) {
#pragma unroll
        for (int i = 0; i < 8; ++i)
#pragma unroll
          for (int j = 0; j < 4; ++j) acc[i][j] = mfma(W[j], X[i], acc[i][j]);
      };
      auto pattern = [&]() {
#pragma unroll
        for (int i = 0; i < 16; ++i) { __builtin_amdgcn_sched_group_barrier(0x008, 1, 0); __builtin_amdgcn_sched_group_barrier(0x100, 1, 0); }
        __builtin_amdgcn_sched_group_barrier(0x008, 16, 0);
      };
      BAR();
      ldf(X0, W0, ax0, aw0, aw1, 0);
      SCHED();
      int buf = 0;
#pragma unroll 1
      for (int s = 0; s < 9; ++s) {
        ldf(X1, W1, ax1, aw0, aw1, 4096);
        mma(X0, W0);
        pattern();
        SCHED();
        WAITL(0);
        BAR();
        const int dlt = buf == 2 ? -2 * STG : STG;
        buf = buf == 2 ? 0 : buf + 1;
        ax0 += dlt; ax1 += dlt; aw0 += dlt; aw1 += dlt;
        SCHED();
        ldf(X0, W0, ax0, aw0, aw1, 0);
        mma(X1, W1);
        pattern();
        SCHED();
      }
      ldf(X1, W1, ax1, aw0, aw1, 4096);
      mma(X0, W0);
      pattern();
      SCHED();
      mma(X1, W1);
      SCHED();
    }
    BAR();
    __builtin_amdgcn_s_setprio(2);
#pragma unroll
    for (int k = 0; k < WPRE; ++k)
#pragma unroll
      for (int j = 0; j < 5; ++j) W2[k][j] = __builtin_bit_cast(bf16x8, ld_sc1(rws, lane * 16, wso + k * 5120 + j * 1024));
    SCHED();
    f32x4 cs[4];
#pragma unroll
    for (int j = 0; j < 4; ++j) cs[j] = *(LAS f32x4*)(lds + L_CS + ((j >> 1) * 64 + wc * 32 + (j & 1) * 16 + 4 * g) * 4);
    const u32 hso = OFF_HT + ((rt * 16 + wr * 8) * 54 + ct * 2 + wc) * 1024;
    const float K1 = -2.0f * 0.7978845608028654f * 1.4426950408889634f, K2 = K1 * 0.044715f;
#pragma unroll
    for (int i = 0; i < 8; ++i) {
      const f32x4 st = *(LAS f32x4*)(lds + L_ST + (wr * 128 + i * 16 + tl) * 16);
      const f32x2 nm = {-st.x, -st.x}, rs = {st.y, st.y}, sd = {st.z, st.z};
      u32x4 o;
#pragma unroll
      for (int j = 0; j < 2; ++j)
#pragma unroll
        for (int h = 0; h < 2; ++h) {
          f32x2 ag = {acc[i][j][2 * h], acc[i][j][2 * h + 1]}, au = {acc[i][2 + j][2 * h], acc[i][2 + j][2 * h + 1]};
          f32x2 cgv = {cs[j][2 * h], cs[j][2 * h + 1]}, cuv = {cs[2 + j][2 * h], cs[2 + j][2 * h + 1]};
          f32x2 gv = __builtin_elementwise_fma(nm, cgv, ag) * rs;
          f32x2 uv = __builtin_elementwise_fma(nm, cuv, au);
          f32x2 pv = __builtin_elementwise_fma(gv * gv, f32x2{K2, K2}, f32x2{K1, K1}) * gv;
          f32x2 e = {__builtin_amdgcn_exp2f(pv.x), __builtin_amdgcn_exp2f(pv.y)};
          f32x2 dn = __builtin_elementwise_fma(e, sd, sd);
          f32x2 r = {__builtin_amdgcn_rcpf(dn.x), __builtin_amdgcn_rcpf(dn.y)};
          o[j * 2 + h] = pk2v(gv * uv * r);
        }
      st_sc1(rws, o, lane * 16, hso + i * (54 * 1024));
    }
    WAITV(0);
    const u32 T = __builtin_amdgcn_readfirstlane(*(LAS u32*)(lds + L_T));
    if (lane == 0) stflag(ws, FL_H + ((rt * 2 + wr) * 64 + ct * 2 + wc) * 4, T);
  } else {
      aux_work(lds, ws, x, kk, vv, shk, wd, aux, wid, lane);
#pragma unroll
      for (int k = 0; k < WPRE; ++k)
#pragma unroll
        for (int j = 0; j < 5; ++j) W2[k][j] = __builtin_bit_cast(bf16x8, ld_sc1(rws, lane * 16, wso + k * 5120 + j * 1024));
    }
    const u32 T = __builtin_amdgcn_readfirstlane(*(LAS u32*)(lds + L_T));
    const int w = wid;
    const int t = rq * 64 + w * 16 + tl;
    u32x2 xv[5];
#pragma unroll
    for (int j = 0; j < 5; ++j) xv[j] = *(const GAS u32x2*)((const char*)x + (size_t)t * 1280 + (c2 * 80 + j * 16 + 4 * g) * 2);
    const u32x4 stv = ld_sc1(rws, t * 16, OFF_ST);
    const int nk = w < 2 ? 14 : 13;
    wait_flags(ws, FL_H + ((rq >> 1) * 64 + ks0 + (lane < nk ? lane : 0)) * 4, lane < nk, T);
    const u32 hso = OFF_HT + (rq * 4 * 54 + ks0) * 1024;
    u32 ho[4];
#pragma unroll
    for (int i = 0; i < 4; ++i) ho[i] = ((w + i) & 3) * (54 * 1024) + lane * 16;
    f32x4 acc[4][5];
#pragma unroll
    for (int i = 0; i < 4; ++i)
#pragma unroll
      for (int j = 0; j < 5; ++j) acc[i][j] = f32x4{0.f, 0.f, 0.f, 0.f};
    {
      constexpr int R = 4, NK = 14;
      bf16x8 H[R][4];
      const rsrc_t rl = mkrsrc(ws, w < 2 ? WS_USED : 0);
      auto ldh = [&](int k) {
#pragma unroll
        for (int i = 0; i < 4; ++i) H[k % R][i] = __builtin_bit_cast(bf16x8, ld_sc1(k == NK - 1 ? rl : rws, ho[i], hso + k * 1024));
      };
      auto ldw = [&](int k) {
#pragma unroll
        for (int j = 0; j < 5; ++j) W2[k % WPRE][j] = __builtin_bit_cast(bf16x8, ld_sc1(k == NK - 1 ? rl : rws, lane * 16, wso + k * 5120 + j * 1024));
      };
#pragma unroll
      for (int k = 0; k < R; ++k) { ldh(k); SCHED(); }
#pragma unroll
      for (int k = 0; k < NK; ++k) {
#pragma unroll
        for (int i = 0; i < 4; ++i)
#pragma unroll
          for (int j = 0; j < 5; ++j) acc[i][j] = mfma(W2[k % WPRE][j], H[k % R][i], acc[i][j]);
        SCHED();
        if (k + WPRE < NK) ldw(k + WPRE);
        if (k + R < NK) ldh(k + R);
        SCHED();
      }
    }
    auto redp = [&](int dest, int src, int j) { return lds + (dest == 3 ? 0 : L_RED + dest * 15360) + (src * 5 + j) * 1024 + lane * 16; };
#pragma unroll
    for (int i = 1; i < 4; ++i)
#pragma unroll
      for (int j = 0; j < 5; ++j) *(LAS f32x4*)redp((w + i) & 3, i - 1, j) = acc[i][j];
    WAITL(0);
    BAR();
    const float mean = __uint_as_float(stv.x), rstd = __uint_as_float(stv.y);
#pragma unroll
    for (int j = 0; j < 5; ++j) {
      f32x4 a = acc[0][j];
#pragma unroll
      for (int i = 0; i < 3; ++i) a += *(LAS f32x4*)redp(w, i, j);
      const int row = j * 16 + 4 * g + q;
      u32x2 z = trd(lds + L_Z + row * 128 + (((w * 2 + (p >> 1)) ^ (((row >> 1) & 3) << 1)) << 4) + (p & 1) * 8);
      float xs[4] = {bflo(xv[j].x), bfhi(xv[j].x), bflo(xv[j].y), bfhi(xv[j].y)};
      float zs[4] = {bflo(z.x), bfhi(z.x), bflo(z.y), bfhi(z.y)};
      float o[4];
#pragma unroll
      for (int r = 0; r < 4; ++r) o[r] = xs[r] + (xs[r] - mean) * rstd * zs[r] + a[r];
      u32x2 ov = {pk2(o[0], o[1]), pk2(o[2], o[3])};
      *(GAS u32x2*)((char*)out + (size_t)t * 1280 + (c2 * 80 + j * 16 + 4 * g) * 2) = ov;
    }
  } else {
    const int lw = wid - 4;
    if (isg) {
      const char* xb = (const char*)x + (size_t)(rt * 256 + lw * 64) * 1280;
      const u32 xo0 = (lane >> 3) * 1280 + (((lane & 7) ^ (lane >> 4)) << 4);
      const u32 xo1 = (lane >> 3) * 1280 + (((lane & 7) ^ (4 | (lane >> 4))) << 4);
      const char* wb = (const char*)((lw >> 1) ? wu : wg) + ct * 128 + (lw & 1) * 32 * 3456;
      const u32 wo0 = (lane >> 3) * 3456 + (((lane & 7) ^ (((lane >> 4) & 1) << 1)) << 4);
      const u32 wo1 = (lane >> 3) * 3456 + (((lane & 7) ^ ((((lane >> 4) & 1) | 2) << 1)) << 4);
      auto issue = [&](int s, int buf) {
        LAS char* lx = lds + buf * STG + lw * 8192;
        LAS char* lwp = lds + buf * STG + 32768 + lw * 4096;
        const char* xs = xb + s * 128;
        const char* wsrc = wb + s * (64 * 3456);
#pragma unroll
        for (int i = 0; i < 4; ++i) glds(wsrc + i * (8 * 3456) + ((i & 1) ? wo1 : wo0), lwp + i * 1024);
#pragma unroll
        for (int i = 0; i < 8; ++i) glds(xs + i * 10240 + ((i & 1) ? xo1 : xo0), lx + i * 1024);
      };
      issue(0, 0); issue(1, 1); issue(2, 2);
      const u32 lb = (u32)(uintptr_t)lds;
      const int f = ((q >> 1) & 1) | ((g & 1) << 1);
      const u32 wadr = lb + 32768 + (lw >> 1) * 8192 + (8 * g + q) * 128 + ((((lw & 1) * 4 + (p >> 1)) ^ (f << 1)) << 4) + (p & 1) * 8;
      float c0 = 0.f, c1 = 0.f;
      const u32 ones = 0x3f803f80u;
      int buf = 0;
#pragma unroll 1
      for (int s = 0; s < 10; ++s) {
        if (s == 0) WAITV(24); else if (s == 9) WAITV(0); else WAITV(12);
        BAR();
        const u32 bo = buf * STG;
        u32x2 wv[8];
        const u32 w0 = wadr + bo, w1 = w0 ^ 32;
        asm volatile("ds_read_b64_tr_b16 %0, %1" : "=v"(wv[0]) : "v"(w0));
        asm volatile("ds_read_b64_tr_b16 %0, %1 offset:512" : "=v"(wv[1]) : "v"(w0));
        asm volatile("ds_read_b64_tr_b16 %0, %1 offset:4096" : "=v"(wv[2]) : "v"(w0));
        asm volatile("ds_read_b64_tr_b16 %0, %1 offset:4608" : "=v"(wv[3]) : "v"(w0));
        asm volatile("ds_read_b64_tr_b16 %0, %1" : "=v"(wv[4]) : "v"(w1));
        asm volatile("ds_read_b64_tr_b16 %0, %1 offset:512" : "=v"(wv[5]) : "v"(w1));
        asm volatile("ds_read_b64_tr_b16 %0, %1 offset:4096" : "=v"(wv[6]) : "v"(w1));
        asm volatile("ds_read_b64_tr_b16 %0, %1 offset:4608" : "=v"(wv[7]) : "v"(w1));
        if (s >= 1 && s <= 7) { int nb = buf == 0 ? 2 : buf - 1; issue(s + 2, nb); }
        asm volatile("s_waitcnt lgkmcnt(0)"
                     : "+v"(wv[0]), "+v"(wv[1]), "+v"(wv[2]), "+v"(wv[3]), "+v"(wv[4]), "+v"(wv[5]), "+v"(wv[6]), "+v"(wv[7]));
#pragma unroll
        for (int i = 0; i < 4; ++i) { c0 = dot2(wv[i].x, ones, c0); c0 = dot2(wv[i].y, ones, c0); c1 = dot2(wv[4 + i].x, ones, c1); c1 = dot2(wv[4 + i].y, ones, c1); }
        buf = buf == 2 ? 0 : buf + 1;
      }
      const u32 T = __builtin_amdgcn_readfirstlane(*(LAS u32*)(lds + L_T));
      wait_flags(ws, FL_AUX + (lane < 40 ? lane : 0) * 4, lane < 40, T);
      const u32x4 st = ld_sc1(rws, lane * 16, OFF_ST + (rt * 256 + lw * 64) * 16);
      c0 += __shfl_xor(c0, 16); c0 += __shfl_xor(c0, 32);
      c1 += __shfl_xor(c1, 16); c1 += __shfl_xor(c1, 32);
      if (lane < 16) { *(LAS float*)(lds + L_CS + (lw * 32 + lane) * 4) = c0; *(LAS float*)(lds + L_CS + (lw * 32 + 16 + lane) * 4) = c1; }
      *(LAS u32x4*)(lds + L_ST + (lw * 64 + lane) * 16) = st;
      WAITL(0);
      BAR();
    } else {
      aux_work(lds, ws, x, kk, vv, shk, wd, aux, wid, lane);
    }
    const u32 T = __builtin_amdgcn_readfirstlane(*(LAS u32*)(lds + L_T));
    const int cw = wid - 4;
    const int gidx = rt * 27 + ct;
    int ch = -1;
    if (!isg) ch = aux * 4 + cw;
    else if (cw < 2) ch = 160 + gidx * 2 + cw;
    else if (cw == 2 && gidx < 48) ch = 592 + gidx;
    if (ch >= 0) conv_channel(lds + L_CV + cw * CVSZ, ws, ssk, ssD, ch, lane, T, wid);
    if (cw == 3) {
      for (int spins = 0; spins < SPIN_MAX; ++spins) {
        const u32 v0 = ldflag(ws, FL_Z + (c2 * 128 + lane) * 4), v1 = ldflag(ws, FL_Z + (c2 * 128 + 64 + (lane & 15)) * 4);
        if (__all(v0 == T && v1 == T)) break;
        __builtin_amdgcn_s_sleep(4);
      }
      asm volatile("" ::: "memory");
      u32x4 zv[10];
#pragma unroll
      for (int i = 0; i < 10; ++i) zv[i] = ld_sc1(rws, (lane >> 3) * 4096 + (lane & 7) * 16, OFF_Z + (c2 * 80 + i * 8) * 4096 + rq * 128);
#pragma unroll
      for (int i = 0; i < 10; ++i) { const int row = i * 8 + (lane >> 3); *(LAS u32x4*)(lds + L_Z + row * 128 + (((lane & 7) ^ (((row >> 1) & 3) << 1)) << 4)) = zv[i]; }
    }
    WAITL(0);
    BAR();
    if (cw == 3 && b == 0) {
      for (int spins = 0; spins < SPIN_MAX; ++spins) {
        asm volatile("" ::: "memory");
        const u32x4 v = ld_sc1(rws, lane * 16, FL_ACK);
        if (__all(v.x == T && v.y == T && v.z == T && v.w == T)) break;
        __builtin_amdgcn_s_sleep(4);
      }
      if (lane == 0) stflag(ws, FL_EPOCH, T);
    }
  }
}

extern "C" void kernel_launch(void* const* d_in, const int* in_sizes, int n_in, void* d_out, int out_size, void* d_ws, size_t ws_size,
                              hipStream_t stream) {
  kf<<<256, 512, 0, stream>>>((const u16*)d_in[0], (const u16*)d_in[1], (const u16*)d_in[2], (const u16*)d_in[3], (const u16*)d_in[4],
                              (const u16*)d_in[5], (const u16*)d_in[7], (const u16*)d_in[8], (const u16*)d_in[9], (char*)d_ws, (u16*)d_out);
}
```

```cpp
#include <hip/hip_runtime.h>
#include <hip/hip_bf16.h>

typedef unsigned short u16;
typedef unsigned int u32;
typedef unsigned long long u64;
typedef __attribute__((ext_vector_type(8))) __bf16 bf16x8;
typedef __attribute__((ext_vector_type(2))) __bf16 bf16x2;
typedef __attribute__((ext_vector_type(4))) short s16x4;
typedef __attribute__((ext_vector_type(4))) float f32x4;
typedef __attribute__((ext_vector_type(2))) float f32x2;
typedef __attribute__((ext_vector_type(4))) u32 u32x4;
typedef __attribute__((ext_vector_type(2))) u32 u32x2;
#define GAS __attribute__((address_space(1)))
#define LAS __attribute__((address_space(3)))
#define DI __device__ __forceinline__
#define BAR() __builtin_amdgcn_s_barrier()
#define WAITV(n) asm volatile("s_waitcnt vmcnt(" #n ")" ::: "memory")
#define WAITL(n) asm volatile("s_waitcnt lgkmcnt(" #n ")" ::: "memory")
#define SCHED() __builtin_amdgcn_sched_barrier(0)
#define RLX_AGENT __ATOMIC_RELAXED, __HIP_MEMORY_SCOPE_AGENT

constexpr int S = 2048, D = 640, F = 1728;
constexpr size_t OFF_HT = 0;
constexpr size_t OFF_WDT = 8u << 20;
constexpr size_t OFF_UP = 11u << 20;
constexpr size_t OFF_Z = 14u << 20;
constexpr size_t OFF_ST = 17u << 20;
constexpr size_t OFF_FL = (17u << 20) + 65536;

DI float bflo(u32 v) { return __uint_as_float(v << 16); }
DI float bfhi(u32 v) { return __uint_as_float(v & 0xffff0000u); }
DI u32 pk2(float a, float b) { f32x2 v = {a, b}; bf16x2 r = __builtin_convertvector(v, bf16x2); return __builtin_bit_cast(u32, r); }
DI u32 pk2v(f32x2 v) { bf16x2 r = __builtin_convertvector(v, bf16x2); return __builtin_bit_cast(u32, r); }
DI f32x4 mfma(bf16x8 a, bf16x8 b, f32x4 c) { return __builtin_amdgcn_mfma_f32_16x16x32_bf16(a, b, c, 0, 0, 0); }
DI u32x2 trd(LAS char* p) { s16x4 v = __builtin_amdgcn_ds_read_tr16_b64_v4i16((LAS s16x4*)p); return __builtin_bit_cast(u32x2, v); }
DI bf16x8 cat(u32x2 a, u32x2 b) { u32x4 v = {a.x, a.y, b.x, b.y}; return __builtin_bit_cast(bf16x8, v); }
DI void glds(const char* g, LAS char* l) { __builtin_amdgcn_global_load_lds((const GAS u32*)g, (LAS u32*)l, 16, 0, 0); }
DI float dot2(u32 a, u32 b, float c) { return __builtin_amdgcn_fdot2_f32_bf16(__builtin_bit_cast(bf16x2, a), __builtin_bit_cast(bf16x2, b), c, false); }

#define NSTAT 8
#define NOLOAD 0
#define NOMMA 0
#define ROT 0
#define NOLDF 0
constexpr int STG = 49152;
constexpr int L1_ST = 3 * STG;
constexpr int L1_CS = L1_ST + 4096;
constexpr int LDS1 = L1_CS + 512;

__global__ __launch_bounds__(512, 2) void k1(const u16* __restrict__ x, const u16* __restrict__ kk, const u16* __restrict__ vv,
                                             const u16* __restrict__ shk, const u16* __restrict__ wg, const u16* __restrict__ wu,
                                             const u16* __restrict__ wd, char* __restrict__ ws) {
  __shared__ __attribute__((aligned(1024))) char lds_[LDS1];
  LAS char* lds = (LAS char*)lds_;
  const int b = blockIdx.x, xcd = b & 7, slot = b >> 3;
  const int cg = xcd & 3, rg = xcd >> 2;
  const int ncol = cg == 3 ? 6 : 7;
  const int wid = __builtin_amdgcn_readfirstlane(threadIdx.x >> 6), lane = threadIdx.x & 63;
  const int g = lane >> 4, q = (lane >> 2) & 3, p = lane & 3;

  if (slot < 4 * ncol) {
    const int rt = rg * 4 + (slot & 3), ct = cg * 7 + (slot >> 2);
    if (wid >= 4) {
      const int lw = wid - 4;
      const char* xb = (const char*)x + (size_t)(rt * 256 + lw * 64) * 1280;
      const u32 xo0 = (lane >> 3) * 1280 + (((lane & 7) ^ (lane >> 4)) << 4);
      const u32 xo1 = (lane >> 3) * 1280 + (((lane & 7) ^ (4 | (lane >> 4))) << 4);
      const char* wb = (const char*)((lw >> 1) ? wu : wg) + ct * 128 + (lw & 1) * 32 * 3456;
      const u32 wo0 = (lane >> 3) * 3456 + (((lane & 7) ^ (((lane >> 4) & 1) << 1)) << 4);
      const u32 wo1 = (lane >> 3) * 3456 + (((lane & 7) ^ ((((lane >> 4) & 1) | 2) << 1)) << 4);
      const int rot = ROT == 1 ? (slot % 10) : ROT == 2 ? (slot & 1) : ROT == 3 ? ((slot >> 2) + (slot & 3) * 3) % 10 : 0;
      auto issue = [&](int s, int buf) {
        s += rot; if (s >= 10) s -= 10;
        LAS char* lx = lds + buf * STG + lw * 8192;
        LAS char* lwp = lds + buf * STG + 32768 + lw * 4096;
        const char* xs = xb + s * 128;
        const char* wsrc = wb + s * (64 * 3456);
#pragma unroll
        for (int i = 0; i < 4; ++i) glds(wsrc + i * (8 * 3456) + ((i & 1) ? wo1 : wo0), lwp + i * 1024);
#pragma unroll
        for (int i = 0; i < 8; ++i) glds(xs + i * 10240 + ((i & 1) ? xo1 : xo0), lx + i * 1024);
      };
      issue(0, 0); issue(1, 1); issue(2, 2);
      const u32 lb = (u32)(uintptr_t)lds;
      const u32 srow = lb + (lw * 64 + lane) * 128 + (((lane >> 1) & 7) << 4);
      const int f = ((q >> 1) & 1) | ((g & 1) << 1);
      const u32 wadr = lb + 32768 + (lw >> 1) * 8192 + (8 * g + q) * 128 + ((((lw & 1) * 4 + (p >> 1)) ^ (f << 1)) << 4) + (p & 1) * 8;
      float s1 = 0.f, s2 = 0.f, c0 = 0.f, c1 = 0.f;
      const u32 ones = 0x3f803f80u;
      int buf = 0;
#pragma unroll 1
      for (int s = 0; s < 10; ++s) {
        if (s == 0) WAITV(24); else if (s == 9) WAITV(0); else WAITV(12);
        BAR();
        if (s >= 1 && s <= 7 && !NOLOAD) { int nb = buf == 0 ? 2 : buf - 1; issue(s + 2, nb); }
        if (NOLDF & 2) continue;
        const u32 bo = buf * STG;
        u32x4 xv[8]; u32x2 wv[8];
        const u32 a0 = srow + bo, w0 = wadr + bo, w1 = w0 ^ 32;
#pragma unroll
        for (int i = 0; i < 8; ++i) { u32 a = a0 ^ (i << 4); asm volatile("ds_read_b128 %0, %1" : "=v"(xv[i]) : "v"(a)); }
        asm volatile("ds_read_b64_tr_b16 %0, %1" : "=v"(wv[0]) : "v"(w0));
        asm volatile("ds_read_b64_tr_b16 %0, %1 offset:512" : "=v"(wv[1]) : "v"(w0));
        asm volatile("ds_read_b64_tr_b16 %0, %1 offset:4096" : "=v"(wv[2]) : "v"(w0));
        asm volatile("ds_read_b64_tr_b16 %0, %1 offset:4608" : "=v"(wv[3]) : "v"(w0));
        asm volatile("ds_read_b64_tr_b16 %0, %1" : "=v"(wv[4]) : "v"(w1));
        asm volatile("ds_read_b64_tr_b16 %0, %1 offset:512" : "=v"(wv[5]) : "v"(w1));
        asm volatile("ds_read_b64_tr_b16 %0, %1 offset:4096" : "=v"(wv[6]) : "v"(w1));
        asm volatile("ds_read_b64_tr_b16 %0, %1 offset:4608" : "=v"(wv[7]) : "v"(w1));
        asm volatile("s_waitcnt lgkmcnt(0)"
                     : "+v"(xv[0]), "+v"(xv[1]), "+v"(xv[2]), "+v"(xv[3]), "+v"(xv[4]), "+v"(xv[5]), "+v"(xv[6]), "+v"(xv[7]),
                       "+v"(wv[0]), "+v"(wv[1]), "+v"(wv[2]), "+v"(wv[3]), "+v"(wv[4]), "+v"(wv[5]), "+v"(wv[6]), "+v"(wv[7]));
#pragma unroll
        for (int i = 0; i < NSTAT; ++i)
#pragma unroll
          for (int j = 0; j < 4; ++j) { s1 = dot2(xv[i][j], ones, s1); s2 = dot2(xv[i][j], xv[i][j], s2); }
#pragma unroll
        for (int i = 0; i < 4; ++i) { c0 = dot2(wv[i].x, ones, c0); c0 = dot2(wv[i].y, ones, c0); c1 = dot2(wv[4 + i].x, ones, c1); c1 = dot2(wv[4 + i].y, ones, c1); }
        buf = buf == 2 ? 0 : buf + 1;
      }
      const float mean = s1 * (1.f / D);
      const float var = fmaxf(s2 * (1.f / D) - mean * mean, 0.f) + 1e-5f;
      const float rstd = __builtin_amdgcn_rsqf(var);
      const float sd = var * rstd;
      f32x4 st = {mean, rstd, sd, 0.f};
      *(LAS f32x4*)(lds + L1_ST + (lw * 64 + lane) * 16) = st;
      c0 += __shfl_xor(c0, 16); c0 += __shfl_xor(c0, 32);
      c1 += __shfl_xor(c1, 16); c1 += __shfl_xor(c1, 32);
      if (lane < 16) { *(LAS float*)(lds + L1_CS + (lw * 32 + lane) * 4) = c0; *(LAS float*)(lds + L1_CS + (lw * 32 + 16 + lane) * 4) = c1; }
      if (ct == 0) { f32x2 o = {mean, rstd}; *(GAS f32x2*)(ws + OFF_ST + (rt * 256 + lw * 64 + lane) * 8) = o; }
      WAITL(0);
      BAR();
    } else {
      const int wr = wid >> 1, wc = wid & 1;
      const int tl = lane & 15;
      u32 ax0 = (wr * 128 + tl) * 128 + ((g ^ (tl >> 1)) << 4);
      u32 ax1 = ax0 ^ 64;
      const int f = ((q >> 1) & 1) | ((g & 1) << 1);
      u32 aw0 = 32768 + (8 * g + q) * 128 + (((wc * 4 + (p >> 1)) ^ (f << 1)) << 4) + (p & 1) * 8;
      u32 aw1 = aw0 ^ 32;
      f32x4 acc[8][4];
#pragma unroll
      for (int i = 0; i < 8; ++i)
#pragma unroll
        for (int j = 0; j < 4; ++j) acc[i][j] = f32x4{0.f, 0.f, 0.f, 0.f};
      bf16x8 X0[8], W0[4], X1[8], W1[4];
      auto ldf = [&](bf16x8(&X)[8], bf16x8(&W)[4], u32 ax, u32 wa, u32 wb2, int ko) {
        W[0] = cat(trd(lds + wa + ko), trd(lds + wa + ko + 512));
        X[0] = *(LAS bf16x8*)(lds + ax);
        W[1] = cat(trd(lds + wb2 + ko), trd(lds + wb2 + ko + 512));
        W[2] = cat(trd(lds + wa + ko + 8192), trd(lds + wa + ko + 8192 + 512));
        W[3] = cat(trd(lds + wb2 + ko + 8192), trd(lds + wb2 + ko + 8192 + 512));
#pragma unroll
        for (int i = 1; i < 8; ++i) X[i] = *(LAS bf16x8*)(lds + ax + i * 2048);
      };
      auto mma = [&](bf16x8(&X)[8], bf16x8(&W)[4]) {
#pragma unroll
        for (int i = 0; i < 8; ++i)
#pragma unroll
          for (int j = 0; j < 4; ++j) acc[i][j] = mfma(W[j], X[i], acc[i][j]);
      };
      auto pattern = [&]() {
#pragma unroll
        for (int i = 0; i < 16; ++i) { __builtin_amdgcn_sched_group_barrier(0x008, 1, 0); __builtin_amdgcn_sched_group_barrier(0x100, 1, 0); }
        __builtin_amdgcn_sched_group_barrier(0x008, 16, 0);
      };
      BAR();

      ldf(X0, W0, ax0, aw0, aw1, 0);
      SCHED();
      int buf = 0;
#pragma unroll 1
      for (int s = 0; s < 9; ++s) {
        if (!NOMMA) {
        if (!(NOLDF&1) || s == 0) ldf(X1, W1, ax1, aw0, aw1, 4096);
        mma(X0, W0);
        pattern(); }
        SCHED();
        WAITL(0);
        BAR();
        const int dlt = buf == 2 ? -2 * STG : STG;
        buf = buf == 2 ? 0 : buf + 1;
        ax0 += dlt; ax1 += dlt; aw0 += dlt; aw1 += dlt;
        SCHED();
        if (!NOMMA) {
        if (!(NOLDF&1)) ldf(X0, W0, ax0, aw0, aw1, 0);
        mma(X1, W1);
        pattern(); }
        SCHED();
      }
      ldf(X1, W1, ax1, aw0, aw1, 4096);
      mma(X0, W0);
      pattern();
      SCHED();
      mma(X1, W1);
      SCHED();

      BAR();

      f32x4 cs[4];
#pragma unroll
      for (int j = 0; j < 4; ++j) cs[j] = *(LAS f32x4*)(lds + L1_CS + ((j >> 1) * 64 + wc * 32 + (j & 1) * 16 + 4 * g) * 4);
      char* hdst = ws + OFF_HT + (size_t)(((rt * 16 + wr * 8) * 54 + ct * 2 + wc) * 1024);
      const float K1 = -2.0f * 0.7978845608028654f * 1.4426950408889634f, K2 = K1 * 0.044715f;
#pragma unroll
      for (int i = 0; i < 8; ++i) {
        const f32x4 st = *(LAS f32x4*)(lds + L1_ST + (wr * 128 + i * 16 + tl) * 16);
        const f32x2 nm = {-st.x, -st.x}, rs = {st.y, st.y}, sd = {st.z, st.z};
        u32x4 o;
#pragma unroll
        for (int j = 0; j < 2; ++j)
#pragma unroll
          for (int h = 0; h < 2; ++h) {
            f32x2 ag = {acc[i][j][2 * h], acc[i][j][2 * h + 1]}, au = {acc[i][2 + j][2 * h], acc[i][2 + j][2 * h + 1]};
            f32x2 cgv = {cs[j][2 * h], cs[j][2 * h + 1]}, cuv = {cs[2 + j][2 * h], cs[2 + j][2 * h + 1]};
            f32x2 gv = __builtin_elementwise_fma(nm, cgv, ag) * rs;
            f32x2 uv = __builtin_elementwise_fma(nm, cuv, au);
            f32x2 pv = __builtin_elementwise_fma(gv * gv, f32x2{K2, K2}, f32x2{K1, K1}) * gv;
            f32x2 e = {__builtin_amdgcn_exp2f(pv.x), __builtin_amdgcn_exp2f(pv.y)};
            f32x2 dn = __builtin_elementwise_fma(e, sd, sd);
            f32x2 r = {__builtin_amdgcn_rcpf(dn.x), __builtin_amdgcn_rcpf(dn.y)};
            o[j * 2 + h] = pk2v(gv * uv * r);
          }
        *(GAS u32x4*)(hdst + (size_t)i * (54 * 1024) + lane * 16) = o;
      }

      WAITV(0);

    }
  } else {
    const int aux = cg < 3 ? (rg * 3 + cg) * 4 + (slot - 28) : 24 + rg * 8 + (slot - 24);
    if (aux == 0 && threadIdx.x < 8) *(GAS u32*)(ws + OFF_FL + threadIdx.x * 256) = 0u;
    const int unit = aux * 2 + (wid >> 2), tq = unit / 10, cq = unit % 10;
    const int w4 = wid & 3, c = lane & 7, rgp = lane >> 3;
    const int t0 = tq * 256 + w4 * 64 + rgp * 8;
    u32x4 kr[11], vr[8], sk[4], wdv[2][4];
    const size_t cofs = (size_t)(cq * 64 + c * 8) * 2;
#pragma unroll
    for (int i = 0; i < 11; ++i) {
      const int t = t0 - 3 + i;
      const int tc = t < 0 ? 0 : t;
      u32x4 v = *(const GAS u32x4*)((const char*)kk + (size_t)tc * 1280 + cofs);
      if (t < 0) v = u32x4{0, 0, 0, 0};
      kr[i] = v;
    }
#pragma unroll
    for (int i = 0; i < 8; ++i) vr[i] = *(const GAS u32x4*)((const char*)vv + (size_t)(t0 + i) * 1280 + cofs);
#pragma unroll
    for (int i = 0; i < 4; ++i) sk[i] = *(const GAS u32x4*)((const char*)shk + cofs * 4 + i * 16);
    const int id0 = aux * 8 + wid, id1 = id0 + 320;
    const int id1c = id1 < 540 ? id1 : id0;
#pragma unroll
    for (int r = 0; r < 2; ++r) {
      const int id = r ? id1c : id0, ks = id / 10, wq = id % 10;
#pragma unroll
      for (int i = 0; i < 4; ++i) wdv[r][i] = *(const GAS u32x4*)((const char*)wd + (size_t)(ks * 32 + i * 8 + rgp) * 1280 + wq * 128 + c * 16);
    }
    LAS char* ub = lds + (wid >> 2) * 32768;
#pragma unroll
    for (int j = 0; j < 8; ++j) {
      float kf[11], tap[4];
#pragma unroll
      for (int i = 0; i < 11; ++i) kf[i] = (j & 1) ? bfhi(kr[i][j >> 1]) : bflo(kr[i][j >> 1]);
      tap[0] = bflo(sk[j >> 1][(j & 1) * 2]); tap[1] = bfhi(sk[j >> 1][(j & 1) * 2]);
      tap[2] = bflo(sk[j >> 1][(j & 1) * 2 + 1]); tap[3] = bfhi(sk[j >> 1][(j & 1) * 2 + 1]);
      float u[8];
#pragma unroll
      for (int i = 0; i < 8; ++i) {
        float a = kf[i] * tap[3]; a = fmaf(kf[i + 1], tap[2], a); a = fmaf(kf[i + 2], tap[1], a); a = fmaf(kf[i + 3], tap[0], a);
        u[i] = a * ((j & 1) ? bfhi(vr[i][j >> 1]) : bflo(vr[i][j >> 1]));
      }
#pragma unroll
      for (int r = 0; r < 4; ++r) *(LAS u32*)(ub + (c * 8 + j) * 512 + r * 128 + ((w4 * 8 + rgp + 4 * c) & 31) * 4) = pk2(u[r], u[r + 4]);
    }
    LAS char* wl = lds + 65536 + wid * 4096;
#pragma unroll
    for (int r = 0; r < 2; ++r) {
      const int id = r ? id1c : id0, ks = id / 10, wq = id % 10;
      if (r == 0 || id1 < 540) {
#pragma unroll
        for (int i = 0; i < 4; ++i) { const int row = i * 8 + rgp; *(LAS u32x4*)(wl + row * 128 + ((c ^ (((row >> 1) & 3) << 1)) << 4)) = wdv[r][i]; }
        const int row = 4 * g + q;
#pragma unroll
        for (int dt = 0; dt < 4; ++dt) {
          LAS char* a = wl + row * 128 + (((dt * 2 + (p >> 1)) ^ (((row >> 1) & 3) << 1)) << 4) + (p & 1) * 8;
          u32x2 lo = trd(a), hi = trd(a + 2048);
          const int Dg = wq * 4 + dt, cc = Dg / 5, jj = Dg % 5;
          u32x4 o = {lo.x, lo.y, hi.x, hi.y};
          *(GAS u32x4*)(ws + OFF_WDT + (size_t)(((cc * 54 + ks) * 5 + jj) * 1024) + lane * 16) = o;
        }
      }
    }

    __syncthreads();
#pragma unroll
    for (int it = 0; it < 8; ++it) {
      const int line = w4 * 64 + it * 8 + rgp, ch = line >> 2, r = line & 3;
      u32x4 v = *(LAS u32x4*)(ub + line * 128 + (((c + (ch >> 3)) & 7) << 4));
      *(GAS u32x4*)(ws + OFF_UP + (size_t)(((cq * 64 + ch) * 4 + r) * 1024) + tq * 128 + c * 16) = v;
    }

    WAITV(0);

  }
}

constexpr int L2_RED = 0;
constexpr int L2_Z = 61440;
constexpr int L2_CV = 71680;
constexpr int CV_K = 64, CV_U = 4160, CVSZ = 4160 + 4 * 1152;
constexpr int LDS2 = L2_CV + 3 * CVSZ;

typedef __amdgpu_buffer_rsrc_t rsrc_t;
DI rsrc_t mkrsrc(const void* p, u32 n) { return __builtin_amdgcn_make_buffer_rsrc((void*)p, 0, n, 0x00020000); }
DI bf16x8 bld(rsrc_t r, u32 vo, u32 so) { return __builtin_bit_cast(bf16x8, __builtin_amdgcn_raw_buffer_load_b128(r, vo, so, 0)); }
DI void gemm2_loop(f32x4 (&acc)[4][5], const char* hp, const char* wp, const u32 (&ho)[4], u32 lo16, bool full) {
  constexpr int R = 4, NK = 14;
  bf16x8 H[R][4], W[R][5];
  const rsrc_t rh = mkrsrc(hp, 0x7fffffff), rw = mkrsrc(wp, 0x7fffffff);
  const rsrc_t rhl = mkrsrc(hp, full ? 0x7fffffff : 0), rwl = mkrsrc(wp, full ? 0x7fffffff : 0);
  auto ld = [&](int k) {
#pragma unroll
    for (int j = 0; j < 5; ++j) W[k % R][j] = bld(k == NK - 1 ? rwl : rw, lo16, k * 5120 + j * 1024);
#pragma unroll
    for (int i = 0; i < 4; ++i) H[k % R][i] = bld(k == NK - 1 ? rhl : rh, ho[i], k * 1024);
  };
#pragma unroll
  for (int k = 0; k < R; ++k) { ld(k); SCHED(); }
#pragma unroll
  for (int k = 0; k < NK; ++k) {
#pragma unroll
    for (int i = 0; i < 4; ++i)
#pragma unroll
      for (int j = 0; j < 5; ++j) acc[i][j] = mfma(W[k % R][j], H[k % R][i], acc[i][j]);
    SCHED();
    if (k + R < NK) ld(k + R);
    SCHED();
  }
}

__global__ __launch_bounds__(512, 2) void k2(const u16* __restrict__ x, const u16* __restrict__ ssk, const u16* __restrict__ ssD,
                                             char* __restrict__ ws, u16* __restrict__ out) {
  __shared__ __attribute__((aligned(1024))) char lds_[LDS2];
  LAS char* lds = (LAS char*)lds_;
  const int b = blockIdx.x, xcd = b & 7, slot = b >> 3;
  const int rq = (xcd >> 1) * 8 + (slot & 7), c = (xcd & 1) * 4 + (slot >> 3);
  const int wid = __builtin_amdgcn_readfirstlane(threadIdx.x >> 6), lane = threadIdx.x & 63;
  const int g = lane >> 4, q = (lane >> 2) & 3, p = lane & 3, tl = lane & 15;

  if (wid < 4) {
    const int w = wid;
    const int t = rq * 64 + w * 16 + tl;
    u32x2 xv[5];
#pragma unroll
    for (int j = 0; j < 5; ++j) xv[j] = *(const GAS u32x2*)((const char*)x + (size_t)t * 1280 + (c * 80 + j * 16 + 4 * g) * 2);
    const f32x2 st = *(const GAS f32x2*)(ws + OFF_ST + t * 8);
    const int ks0 = w < 2 ? w * 14 : 28 + (w - 2) * 13;
    const char* hp = ws + OFF_HT + (size_t)((rq * 4 * 54 + ks0) * 1024);
    const char* wp = ws + OFF_WDT + (size_t)(((c * 54 + ks0) * 5) * 1024);
    u32 ho[4];
#pragma unroll
    for (int i = 0; i < 4; ++i) ho[i] = ((w + i) & 3) * (54 * 1024) + lane * 16;
    f32x4 acc[4][5];
#pragma unroll
    for (int i = 0; i < 4; ++i)
#pragma unroll
      for (int j = 0; j < 5; ++j) acc[i][j] = f32x4{0.f, 0.f, 0.f, 0.f};
    gemm2_loop(acc, hp, wp, ho, lane * 16, w < 2);

#pragma unroll
    for (int i = 1; i < 4; ++i)
#pragma unroll
      for (int j = 0; j < 5; ++j) *(LAS f32x4*)(lds + L2_RED + ((((w + i) & 3) * 3 + (i - 1)) * 5 + j) * 1024 + lane * 16) = acc[i][j];
    WAITL(0);

    BAR();

#pragma unroll
    for (int j = 0; j < 5; ++j) {
      f32x4 a = acc[0][j];
#pragma unroll
      for (int i = 0; i < 3; ++i) a += *(LAS f32x4*)(lds + L2_RED + ((w * 3 + i) * 5 + j) * 1024 + lane * 16);
      const int row = j * 16 + 4 * g + q;
      u32x2 z = trd(lds + L2_Z + row * 128 + (((w * 2 + (p >> 1)) ^ (((row >> 1) & 3) << 1)) << 4) + (p & 1) * 8);
      float xs[4] = {bflo(xv[j].x), bfhi(xv[j].x), bflo(xv[j].y), bfhi(xv[j].y)};
      float zs[4] = {bflo(z.x), bfhi(z.x), bflo(z.y), bfhi(z.y)};
      float o[4];
#pragma unroll
      for (int r = 0; r < 4; ++r) o[r] = xs[r] + (xs[r] - st.x) * st.y * zs[r] + a[r];
      u32x2 ov = {pk2(o[0], o[1]), pk2(o[2], o[3])};
      *(GAS u32x2*)((char*)out + (size_t)t * 1280 + (c * 80 + j * 16 + 4 * g) * 2) = ov;
    }

    WAITV(0);

  } else {
    const int cw = wid - 4;
    const int odd = slot & 1, pidx = (slot >> 1) * 8 + xcd;
    if (cw < 3 - odd) {
      const int ch = pidx * 5 + odd * 3 + cw;
      LAS char* cb = lds + L2_CV + cw * CVSZ;
#pragma unroll
      for (int i = 0; i < 4; ++i) glds((const char*)ssk + (size_t)ch * 4096 + i * 1024 + lane * 16, cb + CV_K + i * 1024);
#pragma unroll
      for (int i = 0; i < 4; ++i) glds(ws + OFF_UP + (size_t)ch * 4096 + i * 1024 + lane * 16, cb + CV_U + i * 1152 + 128);
      const float Dd = bflo(ssD[ch]);
      if (lane < 16) *(LAS u32*)(cb + lane * 4) = 0u;
      *(LAS u32x2*)(cb + CV_U + g * 1152 + tl * 8) = u32x2{0u, 0u};
      WAITV(0);

      bf16x8 A[16];
      {
        LAS char* a = cb + CV_K + (32 * g + 16 - 4 * q + 4 * p) * 2;
#pragma unroll
        for (int s = 0; s < 16; ++s) A[s] = cat(trd(a + s * 256), trd(a + s * 256 - 32));
      }
      f32x4 acc[4][8];
#pragma unroll
      for (int r = 0; r < 4; ++r)
#pragma unroll
        for (int n = 0; n < 8; ++n) acc[r][n] = f32x4{0.f, 0.f, 0.f, 0.f};
      LAS char* ubase = cb + CV_U + 128 + (4 * tl - 8 * g - 4) * 2;
#pragma unroll
      for (int e = -1; e < 15; ++e) {
#pragma unroll
        for (int r = 0; r < 4; ++r) {
          LAS char* a = ubase + r * 1152 + e * 64;
          bf16x8 B = cat(*(LAS u32x2*)a, *(LAS u32x2*)(a + 8));
#pragma unroll
          for (int n = (e + 1) / 2; n < 8; ++n) acc[r][n] = mfma(A[2 * n - e], B, acc[r][n]);
        }
      }
      SCHED();

      const int lam = 4 * tl + g;
      const int sl = (lam + 63) & 63;
      const int srcl = (16 * (sl & 3) + (sl >> 2)) * 4;
      float pv0 = 0.f, pv1 = 0.f, pv2 = 0.f;
      GAS u64* zd = (GAS u64*)(ws + OFF_Z + (size_t)ch * 4096 + lam * 8);
#pragma unroll
      for (int n = 0; n < 8; ++n) {
        const f32x4 d0 = acc[0][n], d1 = acc[1][n], d2 = acc[2][n], d3 = acc[3][n];
        float y0 = d0[0], y1 = d0[1] + d1[0], y2 = d0[2] + d1[1] + d2[0], y3 = d0[3] + d1[2] + d2[1] + d3[0];
        const float cc0 = d1[3] + d2[2] + d3[1], cc1 = d2[3] + d3[2], cc2 = d3[3];
        const float i0 = __int_as_float(__builtin_amdgcn_ds_bpermute(srcl, __float_as_int(cc0)));
        const float i1 = __int_as_float(__builtin_amdgcn_ds_bpermute(srcl, __float_as_int(cc1)));
        const float i2 = __int_as_float(__builtin_amdgcn_ds_bpermute(srcl, __float_as_int(cc2)));
        y0 += lam == 0 ? pv0 : i0; y1 += lam == 0 ? pv1 : i1; y2 += lam == 0 ? pv2 : i2;
        pv0 = i0; pv1 = i1; pv2 = i2;
        LAS char* ua = cb + CV_U + 128 + (64 * n + lam) * 2;
        y0 = fmaf(bflo(*(LAS u16*)(ua)), Dd, y0);
        y1 = fmaf(bflo(*(LAS u16*)(ua + 1152)), Dd, y1);
        y2 = fmaf(bflo(*(LAS u16*)(ua + 2304)), Dd, y2);
        y3 = fmaf(bflo(*(LAS u16*)(ua + 3456)), Dd, y3);
        const u64 o = (u64)pk2(y0, y1) | ((u64)pk2(y2, y3) << 32);
        __hip_atomic_store(zd + n * 64, o, RLX_AGENT);
      }

      WAITV(0);

      if (lane == 0) __hip_atomic_fetch_add((GAS u32*)(ws + OFF_FL + (ch / 80) * 256), 1u, RLX_AGENT);
    }
    if (cw == 3) {
      GAS u32* fl = (GAS u32*)(ws + OFF_FL + c * 256);
      for (int spins = 0; spins < (1 << 20); ++spins) {
        if ((u32)__builtin_amdgcn_readfirstlane(__hip_atomic_load(fl, RLX_AGENT)) >= 80u) break;
        __builtin_amdgcn_s_sleep(4);
      }

      __builtin_amdgcn_fence(__ATOMIC_ACQUIRE, "agent");
      u32x4 zv[10];
#pragma unroll
      for (int i = 0; i < 10; ++i) zv[i] = *(const GAS u32x4*)(ws + OFF_Z + (size_t)(c * 80 + i * 8 + (lane >> 3)) * 4096 + rq * 128 + (lane & 7) * 16);
#pragma unroll
      for (int i = 0; i < 10; ++i) { const int row = i * 8 + (lane >> 3); *(LAS u32x4*)(lds + L2_Z + row * 128 + (((lane & 7) ^ (((row >> 1) & 3) << 1)) << 4)) = zv[i]; }

    }
    WAITL(0);
    BAR();
  }
}

extern "C" void kernel_launch(void* const* d_in, const int* in_sizes, int n_in, void* d_out, int out_size, void* d_ws, size_t ws_size,
                              hipStream_t stream) {
  const u16* q = (const u16*)d_in[0]; const u16* k = (const u16*)d_in[1]; const u16* v = (const u16*)d_in[2];
  const u16* shk = (const u16*)d_in[3]; const u16* ssk = (const u16*)d_in[4]; const u16* ssD = (const u16*)d_in[5];
  const u16* wg = (const u16*)d_in[7]; const u16* wu = (const u16*)d_in[8]; const u16* wd = (const u16*)d_in[9];
  k1<<<256, 512, 0, stream>>>(q, k, v, shk, wg, wu, wd, (char*)d_ws);
  k2<<<256, 512, 0, stream>>>(q, ssk, ssD, (char*)d_ws, (u16*)d_out);
}
```

```cpp
#include <hip/hip_runtime.h>
#include <hip/hip_bf16.h>

typedef unsigned short u16;
typedef unsigned int u32;
typedef unsigned long long u64;
typedef __attribute__((ext_vector_type(8))) __bf16 bf16x8;
typedef __attribute__((ext_vector_type(2))) __bf16 bf16x2;
typedef __attribute__((ext_vector_type(4))) short s16x4;
typedef __attribute__((ext_vector_type(4))) float f32x4;
typedef __attribute__((ext_vector_type(2))) float f32x2;
typedef __attribute__((ext_vector_type(4))) u32 u32x4;
typedef __attribute__((ext_vector_type(2))) u32 u32x2;
typedef __amdgpu_buffer_rsrc_t rsrc_t;
#define GAS __attribute__((address_space(1)))
#define LAS __attribute__((address_space(3)))
#define DI __device__ __forceinline__
#define BAR() asm volatile("s_barrier" ::: "memory")
#define WAITV(n) asm volatile("s_waitcnt vmcnt(" #n ")" ::: "memory")
#define WAITL(n) asm volatile("s_waitcnt lgkmcnt(" #n ")" ::: "memory")
#define SCHED() __builtin_amdgcn_sched_barrier(0)
#define RLX_AGENT __ATOMIC_RELAXED, __HIP_MEMORY_SCOPE_AGENT

constexpr int S = 2048, D = 640, F = 1728;
constexpr u32 OFF_HT = 0;
constexpr u32 OFF_WDT = 8u << 20;
constexpr u32 OFF_UP = 11u << 20;
constexpr u32 OFF_Z = 14u << 20;
constexpr u32 OFF_ST = 17u << 20;
constexpr u32 OFF_FL = (17u << 20) + 65536;
constexpr u32 FL_EPOCH = OFF_FL;
constexpr u32 FL_A = OFF_FL + 1024;
constexpr u32 FL_H = OFF_FL + 4096;
constexpr u32 FL_Z = OFF_FL + 8192;
constexpr u32 WS_USED = OFF_FL + 16384;
constexpr int SPIN_MAX = 1 << 18;
constexpr int AUXCH = 6;
constexpr int G2CH = 640 - 40 * AUXCH - 216;

DI float bflo(u32 v) { return __uint_as_float(v << 16); }
DI float bfhi(u32 v) { return __uint_as_float(v & 0xffff0000u); }
DI u32 pk2(float a, float b) { f32x2 v = {a, b}; bf16x2 r = __builtin_convertvector(v, bf16x2); return __builtin_bit_cast(u32, r); }
DI u32 pk2v(f32x2 v) { bf16x2 r = __builtin_convertvector(v, bf16x2); return __builtin_bit_cast(u32, r); }
DI f32x4 mfma(bf16x8 a, bf16x8 b, f32x4 c) { return __builtin_amdgcn_mfma_f32_16x16x32_bf16(a, b, c, 0, 0, 0); }
DI u32x2 trd(LAS char* p) { s16x4 v = __builtin_amdgcn_ds_read_tr16_b64_v4i16((LAS s16x4*)p); return __builtin_bit_cast(u32x2, v); }
DI bf16x8 cat(u32x2 a, u32x2 b) { u32x4 v = {a.x, a.y, b.x, b.y}; return __builtin_bit_cast(bf16x8, v); }
DI float dot2(u32 a, u32 b, float c) { return __builtin_amdgcn_fdot2_f32_bf16(__builtin_bit_cast(bf16x2, a), __builtin_bit_cast(bf16x2, b), c, false); }
DI rsrc_t mkrsrc(const void* p, u32 n) { return __builtin_amdgcn_make_buffer_rsrc((void*)p, 0, n, 0x00020000); }
DI u32x4 ld_sc1(rsrc_t r, u32 vo, u32 so) { return __builtin_amdgcn_raw_buffer_load_b128(r, vo, so, 16); }
DI void st_sc1(rsrc_t r, u32x4 v, u32 vo) { __builtin_amdgcn_raw_buffer_store_b128(v, r, vo, 0, 16); }
DI u32 ldflag(char* ws, u32 off) { return __hip_atomic_load((GAS u32*)(ws + off), RLX_AGENT); }
DI void stflag(char* ws, u32 off, u32 v) { __hip_atomic_store((GAS u32*)(ws + off), v, RLX_AGENT); }
DI void glds(const void* sbase, u32 voff, u32 la) {
  asm volatile("s_mov_b32 m0, %2\n\ts_nop 0\n\tglobal_load_lds_dwordx4 %0, %1" ::"v"(voff), "s"(sbase), "s"(la) : "memory");
}
DI void glds_sc1(const void* sbase, u32 voff, u32 la) {
  asm volatile("s_mov_b32 m0, %2\n\ts_nop 0\n\tglobal_load_lds_dwordx4 %0, %1 sc1" ::"v"(voff), "s"(sbase), "s"(la) : "memory");
}
DI void aload16(u32x4& v, const void* sbase, u32 voff) { asm volatile("global_load_dwordx4 %0, %1, %2" : "=v"(v) : "v"(voff), "s"(sbase) : "memory"); }
DI void aload16_sc1(u32x4& v, const void* sbase, u32 voff) { asm volatile("global_load_dwordx4 %0, %1, %2 sc1" : "=v"(v) : "v"(voff), "s"(sbase) : "memory"); }
DI void aload4_sc1(u32& v, const void* sbase, u32 voff) { asm volatile("global_load_dword %0, %1, %2 sc1" : "=v"(v) : "v"(voff), "s"(sbase) : "memory"); }
DI void aload2(u32& v, const void* sbase, u32 voff) { asm volatile("global_load_ushort %0, %1, %2" : "=v"(v) : "v"(voff), "s"(sbase) : "memory"); }
DI void touch(const void* sbase, u32 voff, u32 la) {
  asm volatile("s_mov_b32 m0, %2\n\ts_nop 0\n\tglobal_load_lds_dword %0, %1" ::"v"(voff), "s"(sbase), "s"(la) : "memory");
}
DI void wait_flags(char* ws, u32 off, bool act, u32 T) {
#pragma unroll 1
  for (int spins = 0; spins < SPIN_MAX; ++spins) {
    const u32 v = ldflag(ws, off);
    if (__all(!act || v == T)) break;
    __builtin_amdgcn_s_sleep(2);
  }
  asm volatile("" ::: "memory");
}
DI void wait_lds(LAS char* p, u32 val) {
#pragma unroll 1
  for (int spins = 0; spins < (SPIN_MAX << 6); ++spins) {
    if (__builtin_amdgcn_readfirstlane(*(volatile LAS u32*)p) == val) break;
    __builtin_amdgcn_s_sleep(1);
  }
  asm volatile("" ::: "memory");
}
DI bool early_ok(u32x4 v, int lane, u32 T, int need) {
  const bool ga = (lane >> 1) < 28, gb = (lane >> 1) < 24;
  const bool a = ga ? (need & 1) : (need & 2), bq = gb ? (need & 1) : (need & 2);
  return __all((!a || (v.x == T && v.y == T && v.z == T)) && (!bq || v.w == T));
}
DI void wait_early(rsrc_t rws, int lane, u32 T, int need) {
#pragma unroll 1
  for (int spins = 0; spins < SPIN_MAX; ++spins) {
    asm volatile("" ::: "memory");
    const u32x4 v = ld_sc1(rws, lane * 16, FL_A);
    if (early_ok(v, lane, T, need)) break;
    __builtin_amdgcn_s_sleep(2);
  }
  asm volatile("" ::: "memory");
}

DI void retry_st(u32x4& st, const char* sbase, u32 vo, u32 T) {
#pragma unroll 1
  for (int spins = 0; spins < SPIN_MAX && !__all(st.y == T && st.w == T); ++spins)
    asm volatile("global_load_dwordx4 %0, %1, %2 sc1\n\ts_waitcnt vmcnt(0)" : "=v"(st) : "v"(vo), "s"(sbase) : "memory");
}

constexpr int STG = 49152;
constexpr int L_T = 3 * STG;
constexpr int L_PF = L_T + 16, L_WF = L_T + 64, L_DUM = L_T + 256;
constexpr int L_CS = L_T + 512;
constexpr int L_ST = L_T + 1536;
constexpr int L_Z = L_ST + 4096;
constexpr int L_RT = L_ST;
constexpr int LDS_SZ = L_Z + 10240;
constexpr int CV_K = 64, CV_U = 4160, CVSZ = 8192;
constexpr int L_CV = 2 * STG;
constexpr int L_W1 = 57344, L_W3 = L_CV + 4 * CVSZ;

DI void conv_ldk(u32x4 (&kin)[4], u32& dd, const u16* ssk, const u16* ssD, int ch, int lane) {
#pragma unroll
  for (int i = 0; i < 4; ++i) kin[i] = *(const GAS u32x4*)((const char*)ssk + (size_t)ch * 4096 + i * 1024 + lane * 16);
  dd = *(const GAS u16*)(ssD + ch);
}
DI void conv_ldu(u32x4 (&uin)[2], rsrc_t rws, int ch, int h, int lane) {
#pragma unroll
  for (int i = 0; i < 2; ++i) uin[i] = ld_sc1(rws, lane * 16, OFF_UP + ch * 4096 + (2 * h + i) * 1024);
}
DI void conv_stage(LAS char* cb, u32x4 (&kin)[4], u32 dd, u32x4 (&uin)[2], int lane) {
  if (lane == 0) kin[0].x = (kin[0].x & 0xffff0000u) | (pk2(bflo(kin[0].x) + bflo(dd), 0.f) & 0xffffu);
  if (lane < 16) *(LAS u32*)(cb + lane * 4) = 0u;
  if (lane < 32) *(LAS u32x2*)(cb + CV_U + (lane >> 4) * 1152 + (lane & 15) * 8) = u32x2{0u, 0u};
#pragma unroll
  for (int i = 0; i < 4; ++i) *(LAS u32x4*)(cb + CV_K + i * 1024 + lane * 16) = kin[i];
#pragma unroll
  for (int i = 0; i < 2; ++i) *(LAS u32x4*)(cb + CV_U + i * 1152 + 128 + lane * 16) = uin[i];
}
template <class GATE, class MID>
DI void conv_run(LAS char* lds, LAS char* cb, LAS char* pb, int me, int partner, u32 seq, char* ws, rsrc_t rws, int ch, int h, int lane, u32 T, int wid,
                 GATE gate, MID mid) {
  const int g = lane >> 4, q = (lane >> 2) & 3, p = lane & 3, tl = lane & 15;
  bf16x8 A[16];
  {
    LAS char* a = cb + CV_K + (32 * g + 16 - 4 * q + 4 * p) * 2;
#pragma unroll
    for (int s = 0; s < 16; ++s) A[s] = cat(trd(a + s * 256), trd(a + s * 256 - 32));
  }
  f32x4 acc[2][8];
#pragma unroll
  for (int r = 0; r < 2; ++r)
#pragma unroll
    for (int n = 0; n < 8; ++n) acc[r][n] = f32x4{0.f, 0.f, 0.f, 0.f};
  LAS char* ubase = cb + CV_U + 128 + (4 * tl - 8 * g - 4) * 2;
  constexpr int RB = 4;
  bf16x8 B[RB][2];
  auto ldb = [&](int e) {
#pragma unroll
    for (int r = 0; r < 2; ++r) { LAS char* a = ubase + r * 1152 + e * 64; B[(e + 1) % RB][r] = cat(*(LAS u32x2*)a, *(LAS u32x2*)(a + 8)); }
  };
#pragma unroll
  for (int e = -1; e < RB - 2; ++e) ldb(e);
  SCHED();
  gate();
  SCHED();
#pragma unroll
  for (int e = -1; e < 15; ++e) {
    if (e + RB - 1 < 15) ldb(e + RB - 1);
    SCHED();
#pragma unroll
    for (int r = 0; r < 2; ++r)
#pragma unroll
      for (int n = (e + 1) / 2; n < 8; ++n) acc[r][n] = mfma(A[2 * n - e], B[(e + 1) % RB][r], acc[r][n]);
    SCHED();
  }
  mid();
  const int lam = 4 * tl + g;
  const int sl = (lam + 63) & 63;
  const int srcl = (16 * (sl & 3) + (sl >> 2)) * 4;
  float cin[8];
#pragma unroll
  for (int n = 0; n < 8; ++n) cin[n] = __int_as_float(__builtin_amdgcn_ds_bpermute(srcl, __float_as_int(acc[1][n][3])));
  LAS char* pw = cb + ((lam ^ ((lam >> 3) & 7)) << 4);
#pragma unroll
  for (int n = 0; n < 8; ++n) {
    const f32x4 d0 = acc[0][n], d1 = acc[1][n];
    const float c = lam == 0 ? (n ? cin[n ? n - 1 : 0] : 0.f) : cin[n];
    *(LAS f32x4*)(pw + n * 1024) = f32x4{d0[0] + c, d0[1] + d1[0], d0[2] + d1[1], d0[3] + d1[2]};
  }
  WAITL(0);
  if (lane == 0) *(volatile LAS u32*)(lds + L_PF + me * 4) = seq;
  wait_lds(lds + L_PF + partner * 4, seq);
  LAS char* f0 = h ? pb : cb;
  LAS char* f1 = h ? cb : pb;
#pragma unroll
  for (int it = 0; it < 2; ++it) {
    const int m0 = h * 256 + it * 128 + lane * 2, mm = m0 > 0 ? m0 - 1 : 0;
    const int x0 = (m0 ^ ((m0 >> 3) & 7)) << 4, x1 = x0 ^ 16, xm = (mm ^ ((mm >> 3) & 7)) << 4;
    const f32x4 a0 = *(LAS f32x4*)(f0 + x0), a1 = *(LAS f32x4*)(f0 + x1), b0 = *(LAS f32x4*)(f1 + x0);
    f32x2 bm = *(LAS f32x2*)(f1 + xm + 8);
    const f32x2 b1 = *(LAS f32x2*)(f1 + x1);
    if (m0 == 0) bm = f32x2{0.f, 0.f};
    u32x4 o = {pk2(a0[0] + bm[0], a0[1] + bm[1]), pk2(a0[2] + b0[0], a0[3] + b0[1]), pk2(a1[0] + b0[2], a1[1] + b0[3]), pk2(a1[2] + b1[0], a1[3] + b1[1])};
    st_sc1(rws, o, OFF_Z + ch * 4096 + m0 * 8);
  }
  WAITV(0);
  if (lane == 0) stflag(ws, FL_Z + (h * 1024 + ch) * 4, T);
}

DI u32 aux_work(LAS char* lds, char* ws, rsrc_t rws, const u16* x, const u16* kk, const u16* vv, const u16* shk, int b, int aux, int auxr, int rg, int wid, int lane) {
  const int c = lane & 7, rgp = lane >> 3;
  u32 E = 0;
  if (wid == 7) E = ldflag(ws, FL_EPOCH);
  const int unit = aux * 2 + (wid >> 2), tq = unit / 10, cq = unit % 10;
  const int w4 = wid & 3;
  const int t0 = tq * 256 + w4 * 64 + rgp * 8;
  u32x4 kr[11], vr[8], sk[4], xr[10];
  const size_t cofs = (size_t)(cq * 64 + c * 8) * 2;
#pragma unroll
  for (int i = 0; i < 4; ++i) sk[i] = *(const GAS u32x4*)((const char*)shk + cofs * 4 + i * 16);
#pragma unroll
  for (int i = 0; i < 11; ++i) {
    const int t = t0 - 3 + i;
    const int tc = t < 0 ? 0 : t;
    u32x4 v = *(const GAS u32x4*)((const char*)kk + (size_t)tc * 1280 + cofs);
    if (t < 0) v = u32x4{0, 0, 0, 0};
    kr[i] = v;
  }
#pragma unroll
  for (int i = 0; i < 8; ++i) vr[i] = *(const GAS u32x4*)((const char*)vv + (size_t)(t0 + i) * 1280 + cofs);
  const int su = auxr * 8 + wid;
  const int srow = rg * 1024 + (su < 128 ? su : 0) * 8 + rgp;
#pragma unroll
  for (int i = 0; i < 10; ++i) xr[i] = *(const GAS u32x4*)((const char*)x + (size_t)srow * 1280 + i * 128 + c * 16);
  SCHED();
  if (wid == 0 && lane < 32) *(LAS u32*)(lds + L_PF + lane * 4) = 0u;
  if (wid == 7) {
    const u32 T0 = __builtin_amdgcn_readfirstlane(E) * 1664525u + 1013904223u;
    if (lane == 0) *(LAS u32*)(lds + L_T) = T0;
  }
  LAS char* ub = lds + (wid >> 2) * 32768;
#pragma unroll
  for (int jj = 0; jj < 4; ++jj) {
    f32x2 kf[11], tap[4];
#pragma unroll
    for (int i = 0; i < 11; ++i) kf[i] = f32x2{bflo(kr[i][jj]), bfhi(kr[i][jj])};
    tap[0] = f32x2{bflo(sk[jj][0]), bflo(sk[jj][2])}; tap[1] = f32x2{bfhi(sk[jj][0]), bfhi(sk[jj][2])};
    tap[2] = f32x2{bflo(sk[jj][1]), bflo(sk[jj][3])}; tap[3] = f32x2{bfhi(sk[jj][1]), bfhi(sk[jj][3])};
    f32x2 u[8];
#pragma unroll
    for (int i = 0; i < 8; ++i) {
      f32x2 a = kf[i] * tap[3];
      a = __builtin_elementwise_fma(kf[i + 1], tap[2], a); a = __builtin_elementwise_fma(kf[i + 2], tap[1], a); a = __builtin_elementwise_fma(kf[i + 3], tap[0], a);
      u[i] = a * f32x2{bflo(vr[i][jj]), bfhi(vr[i][jj])};
    }
#pragma unroll
    for (int hh = 0; hh < 2; ++hh)
#pragma unroll
      for (int r = 0; r < 4; ++r) *(LAS u32*)(ub + (c * 8 + jj * 2 + hh) * 512 + r * 128 + ((w4 * 8 + rgp + 4 * c) & 31) * 4) = pk2(u[r][hh], u[r + 4][hh]);
  }
  WAITL(0);
  BAR();
  const u32 T = __builtin_amdgcn_readfirstlane(*(LAS u32*)(lds + L_T));
#pragma unroll
  for (int it = 0; it < 8; ++it) {
    const int line = w4 * 64 + it * 8 + rgp, ch = line >> 2, r = line & 3;
    u32x4 v = *(LAS u32x4*)(ub + line * 128 + (((c + (ch >> 3)) & 7) << 4));
    st_sc1(rws, v, OFF_UP + cq * 64 * 4096 + tq * 128 + ((ch * 4 + r) * 1024) + c * 16);
  }
  {
    float s1 = 0.f;
#pragma unroll
    for (int i = 0; i < 10; ++i)
#pragma unroll
      for (int j = 0; j < 4; ++j) s1 = dot2(xr[i][j], 0x3f803f80u, s1);
    s1 += __shfl_xor(s1, 1); s1 += __shfl_xor(s1, 2); s1 += __shfl_xor(s1, 4);
    const float mean = s1 * (1.f / D);
    f32x2 s2 = {0.f, 0.f};
    const f32x2 m2 = {mean, mean};
#pragma unroll
    for (int i = 0; i < 10; ++i)
#pragma unroll
      for (int j = 0; j < 4; ++j) { const f32x2 a = f32x2{bflo(xr[i][j]), bfhi(xr[i][j])} - m2; s2 = __builtin_elementwise_fma(a, a, s2); }
    float sq = s2.x + s2.y;
    sq += __shfl_xor(sq, 1); sq += __shfl_xor(sq, 2); sq += __shfl_xor(sq, 4);
    const float var = sq * (1.f / D) + 1e-5f;
    u32x4 st = {__float_as_uint(mean), T, __float_as_uint(var), T};
    if (c == 0 && su < 128) st_sc1(rws, st, OFF_ST + srow * 16);
  }
  WAITV(0);
  BAR();
  if (wid == 0 && lane == 0) stflag(ws, FL_A + b * 4, T);
  return T;
}

__global__ __launch_bounds__(512, 2) void kf(const u16* __restrict__ x, const u16* __restrict__ kk, const u16* __restrict__ vv,
                                             const u16* __restrict__ shk, const u16* __restrict__ ssk, const u16* __restrict__ ssD,
                                             const u16* __restrict__ wg, const u16* __restrict__ wu, const u16* __restrict__ wd,
                                             char* __restrict__ ws, u16* __restrict__ out) {
  __shared__ __attribute__((aligned(1024))) char lds_[LDS_SZ];
  LAS char* lds = (LAS char*)lds_;
  const u32 lb = (u32)(uintptr_t)lds;
  const int b = blockIdx.x, xcd = b & 7, slot = b >> 3;
  const int cg = xcd & 3, rg = xcd >> 2;
  const int ncol = cg == 3 ? 6 : 7;
  const int wid = __builtin_amdgcn_readfirstlane(threadIdx.x >> 6), lane = threadIdx.x & 63;
  const int g = lane >> 4, q = (lane >> 2) & 3, p = lane & 3, tl = lane & 15;
  const bool isg = slot < 4 * ncol;
  const int auxr = cg < 3 ? cg * 4 + (slot - 28) : 12 + (slot - 24);
  const int aux = rg * 20 + auxr;
  const int rt = rg * 4 + (slot & 3), ct = cg * 7 + (slot >> 2);
  const int gidx = rt * 27 + ct;
  const int rq = xcd * 4 + (slot & 3), c2 = slot >> 2;
  const rsrc_t rws = mkrsrc(ws, WS_USED);
  const int w4 = wid & 3;
  const int ks0 = w4 < 2 ? w4 * 14 : 28 + (w4 - 2) * 13;
  const u32 wso = OFF_WDT + ((c2 * 54 + ks0) * 5) * 1024;
  const u32 sa0 = L_W1 + w4 * 10240, sa2 = w4 * 5120, sa4 = w4 < 3 ? (8 + w4) * 5120 : L_W3, sa5 = w4 < 2 ? L_W3 + (1 + w4) * 5120 : sa0;
  auto wdma = [&](int j, u32 sa) {
#pragma unroll
    for (int i = 0; i < 5; ++i) glds_sc1(ws + wso + ((4 + j) * 5 + i) * 1024, lane * 16, lb + sa + i * 1024);
  };

  if (wid < 4) {
    bf16x8 W2[4][5];
    u32 T;
    auto ldw2 = [&]() {
#pragma unroll
      for (int k = 0; k < 4; ++k)
#pragma unroll
        for (int j = 0; j < 5; ++j) W2[k][j] = __builtin_bit_cast(bf16x8, ld_sc1(rws, lane * 16, wso + k * 5120 + j * 1024));
    };
    if (isg) {
      {
        const int c7 = slot >> 2, r4 = slot & 3;
        const u32 xrow = (rt * 256 + wid * 64 + lane) * 1280;
        const int l56 = lane < 56 ? lane : 55;
        touch((wid >> 1) ? wu : wg, (192 + ((wid & 1) * 56 + l56) * 4 + r4) * 3456 + ct * 128, lb + L_DUM);
        touch(x, xrow + (3 + c7) * 128, lb + L_DUM);
        if (ncol == 6 && c7 == 0) touch(x, xrow + 9 * 128, lb + L_DUM);
        if (wid == 0) {
          const int chA = 40 * AUXCH + gidx, chB = gidx < G2CH ? 40 * AUXCH + 216 + gidx : chA;
          touch(ssk, (lane < 32 ? chA : chB) * 4096 + (lane & 31) * 128, lb + L_DUM);
          if (lane < 32) *(LAS u32*)(lds + L_PF + lane * 4) = 0u;
          WAITL(0);
        }
      }
      const int wr = wid >> 1, wc = wid & 1;
      u32 ax0 = (wr * 128 + tl) * 128 + ((g ^ (tl >> 1)) << 4);
      u32 ax1 = ax0 ^ 64;
      const int f = ((q >> 1) & 1) | ((g & 1) << 1);
      u32 aw0 = 32768 + (8 * g + q) * 128 + (((wc * 4 + (p >> 1)) ^ (f << 1)) << 4) + (p & 1) * 8;
      u32 aw1 = aw0 ^ 32;
      f32x4 acc[8][4];
#pragma unroll
      for (int i = 0; i < 8; ++i)
#pragma unroll
        for (int j = 0; j < 4; ++j) acc[i][j] = f32x4{0.f, 0.f, 0.f, 0.f};
      f32x4 cs[4];
      {
        bf16x8 X0[8], W0[4], X1[8], W1[4];
        auto ldf = [&](bf16x8(&X)[8], bf16x8(&W)[4], u32 ax, u32 wa, u32 wb2, int ko) {
          W[0] = cat(trd(lds + wa + ko), trd(lds + wa + ko + 512));
          X[0] = *(LAS bf16x8*)(lds + ax);
          W[1] = cat(trd(lds + wb2 + ko), trd(lds + wb2 + ko + 512));
          W[2] = cat(trd(lds + wa + ko + 8192), trd(lds + wa + ko + 8192 + 512));
          W[3] = cat(trd(lds + wb2 + ko + 8192), trd(lds + wb2 + ko + 8192 + 512));
#pragma unroll
          for (int i = 1; i < 8; ++i) X[i] = *(LAS bf16x8*)(lds + ax + i * 2048);
        };
        auto mma = [&](bf16x8(&X)[8], bf16x8(&W)[4]) {
#pragma unroll
          for (int i = 0; i < 8; ++i)
#pragma unroll
            for (int j = 0; j < 4; ++j) acc[i][j] = mfma(W[j], X[i], acc[i][j]);
        };
        auto pattern = [&]() {
#pragma unroll
          for (int i = 0; i < 16; ++i) { __builtin_amdgcn_sched_group_barrier(0x008, 1, 0); __builtin_amdgcn_sched_group_barrier(0x100, 1, 0); }
          __builtin_amdgcn_sched_group_barrier(0x008, 16, 0);
        };
        BAR();
        ldf(X0, W0, ax0, aw0, aw1, 0);
        SCHED();
        int buf = 0;
#pragma unroll 1
        for (int s = 0; s < 9; ++s) {
          ldf(X1, W1, ax1, aw0, aw1, 4096);
          mma(X0, W0);
          pattern();
          SCHED();
          WAITL(0);
          BAR();
          const int dlt = buf == 2 ? -2 * STG : STG;
          buf = buf == 2 ? 0 : buf + 1;
          ax0 += dlt; ax1 += dlt; aw0 += dlt; aw1 += dlt;
          SCHED();
          ldf(X0, W0, ax0, aw0, aw1, 0);
          mma(X1, W1);
          pattern();
          SCHED();
        }
        ldf(X1, W1, ax1, aw0, aw1, 4096);
        mma(X0, W0);
        pattern();
        SCHED();
        WAITL(0);
        BAR();
        SCHED();
#pragma unroll
        for (int j = 0; j < 4; ++j) cs[j] = *(LAS f32x4*)(lds + L_CS + ((j >> 1) * 64 + wc * 32 + (j & 1) * 16 + 4 * g) * 4);
        T = __builtin_amdgcn_readfirstlane(*(LAS u32*)(lds + L_T));
        mma(X1, W1);
        SCHED();
      }
      __builtin_amdgcn_s_setprio(2);
      ldw2();
      SCHED();
      const u32 hvo = OFF_HT + ((rt * 16 + wr * 8) * 54 + ct * 2 + wc) * 1024 + lane * 16;
#pragma unroll
      for (int i = 0; i < 8; ++i) {
        const f32x4 st = *(LAS f32x4*)(lds + L_ST + (wr * 128 + i * 16 + tl) * 16);
        const f32x2 nm = {st.x, st.x}, c1 = {st.y, st.y}, c3 = {st.z, st.z}, vr = {st.w, st.w};
        u32x4 o;
#pragma unroll
        for (int j = 0; j < 2; ++j)
#pragma unroll
          for (int h = 0; h < 2; ++h) {
            f32x2 ag = {acc[i][j][2 * h], acc[i][j][2 * h + 1]}, au = {acc[i][2 + j][2 * h], acc[i][2 + j][2 * h + 1]};
            f32x2 cgv = {cs[j][2 * h], cs[j][2 * h + 1]}, cuv = {cs[2 + j][2 * h], cs[2 + j][2 * h + 1]};
            f32x2 a = __builtin_elementwise_fma(nm, cgv, ag);
            f32x2 bb = __builtin_elementwise_fma(nm, cuv, au);
            f32x2 pv = __builtin_elementwise_fma(a * a, c3, c1) * a;
            f32x2 e = {__builtin_amdgcn_exp2f(pv.x), __builtin_amdgcn_exp2f(pv.y)};
            f32x2 dn = __builtin_elementwise_fma(e, vr, vr);
            f32x2 r = {__builtin_amdgcn_rcpf(dn.x), __builtin_amdgcn_rcpf(dn.y)};
            o[j * 2 + h] = pk2v(a * bb * r);
          }
        st_sc1(rws, o, hvo + i * (54 * 1024));
      }
      SCHED();
      BAR();
      WAITV(0);
      if (lane == 0) stflag(ws, FL_H + ((rt * 2 + wr) * 64 + ct * 2 + wc) * 4, T);
    } else {
      u32x4 kin[4], uin[2];
      u32 dd;
      const int ch = aux * AUXCH + (wid >> 1);
      T = aux_work(lds, ws, rws, x, kk, vv, shk, b, aux, auxr, rg, wid, lane);
      conv_ldk(kin, dd, ssk, ssD, ch, lane);
      wait_early(rws, lane, T, 2);
      conv_ldu(uin, rws, ch, wid & 1, lane);
      conv_stage(lds + wid * CVSZ, kin, dd, uin, lane);
      conv_run(lds, lds + wid * CVSZ, lds + (wid ^ 1) * CVSZ, wid, wid ^ 1, 1u, ws, rws, ch, wid & 1, lane, T, wid, [] {}, [] {});
      wait_early(rws, lane, T, 1);
      BAR();
      wdma(0, sa0); wdma(1, sa0 + 5120); wdma(2, sa2); wdma(3, sa2 + 20480); wdma(4, sa4);
      if (wid < 2) wdma(5, sa5);
      ldw2();
      WAITV(0);
      if (lane == 0) *(volatile LAS u32*)(lds + L_WF + wid * 4) = 1u;
    }
    const int w = wid;
    const int nk = w < 2 ? 14 : 13;
    wait_flags(ws, FL_H + ((rq >> 1) * 64 + ks0 + (lane < nk ? lane : 0)) * 4, lane < nk, T);
    const u32 hso = OFF_HT + (rq * 4 * 54 + ks0) * 1024;
    const u32 adj = w < 2 ? 0u : 1u;
    const u32 hso2 = hso - adj * 1024, wso2 = wso - adj * 5120;
    u32 ho[4];
#pragma unroll
    for (int i = 0; i < 4; ++i) ho[i] = ((w + i) & 3) * (54 * 1024) + lane * 16;
    f32x4 acc[4][5];
#pragma unroll
    for (int i = 0; i < 4; ++i)
#pragma unroll
      for (int j = 0; j < 5; ++j) acc[i][j] = f32x4{0.f, 0.f, 0.f, 0.f};
    const int t = rq * 64 + w * 16 + tl;
    u32x2 xv[5];
    u32x4 stv;
    {
      bf16x8 H[14][4], W3[4][5];
      const rsrc_t rl = mkrsrc(ws, w < 2 ? WS_USED : 0);
      auto ldh = [&](int k) {
#pragma unroll
        for (int i = 0; i < 4; ++i) H[k][i] = __builtin_bit_cast(bf16x8, ld_sc1(k == 9 ? rl : rws, ho[i], (k >= 10 ? hso2 : hso) + k * 1024));
      };
      auto ldw = [&](int k) {
#pragma unroll
        for (int j = 0; j < 5; ++j) W3[k - 10][j] = __builtin_bit_cast(bf16x8, ld_sc1(rws, lane * 16, wso2 + k * 5120 + j * 1024));
      };
      auto step = [&](int k, bf16x8(&W)[5]) {
#pragma unroll
        for (int j = 0; j < 5; ++j)
#pragma unroll
          for (int i = 0; i < 4; ++i) acc[i][j] = mfma(W[j], H[k][i], acc[i][j]);
      };
      ldh(0); ldh(1); ldh(2); ldh(3);
      SCHED();
      step(0, W2[0]); SCHED(); ldh(4); ldh(5); SCHED();
      step(1, W2[1]); SCHED(); ldh(6); ldh(7); SCHED();
      step(2, W2[2]); SCHED(); ldh(8); ldh(9); SCHED();
      wait_lds(lds + L_WF + w * 4, 1u);
      bf16x8 WL[30];
      const u32 sav[6] = {sa0, sa0 + 5120, sa2, sa2 + 20480, sa4, sa5};
      auto ldl = [&](int f) { WL[f] = *(LAS bf16x8*)(lds + sav[f / 5] + (f % 5) * 1024 + lane * 16); };
      ldl(0); ldl(1); ldl(2);
      SCHED();
      step(3, W2[3]); SCHED(); ldw(10); ldh(10); SCHED();
#pragma unroll
      for (int k = 4; k < 10; ++k) {
#pragma unroll
        for (int j = 0; j < 5; ++j) {
          const int f = (k - 4) * 5 + j;
#pragma unroll
          for (int i = 0; i < 4; ++i) acc[i][j] = mfma(WL[f], H[k][i], acc[i][j]);
          if (f + 3 < 30) ldl(f + 3);
          SCHED();
        }
        if (k == 4) ldh(11);
        if (k == 5) ldw(11);
        if (k == 6) ldh(12);
        if (k == 7) ldw(12);
        if (k == 8) ldh(13);
        if (k == 9) ldw(13);
        SCHED();
      }
      step(10, W3[0]); SCHED();
#pragma unroll
      for (int j = 0; j < 5; ++j) xv[j] = *(const GAS u32x2*)((const char*)x + (size_t)t * 1280 + (c2 * 80 + j * 16 + 4 * g) * 2);
      stv = ld_sc1(rws, t * 16, OFF_ST);
      SCHED();
      step(11, W3[1]); SCHED();
      step(12, W3[2]); SCHED();
      step(13, W3[3]); SCHED();
    }
    auto slotp = [&](int ww, int j) { return lds + (j == 2 ? ww * 5120 : L_W1 + ww * 10240 + j * 5120) + lane * 16; };
#pragma unroll
    for (int i = 1; i < 4; ++i)
#pragma unroll
      for (int j = 0; j < 5; ++j) *(LAS f32x4*)(slotp(w, i - 1) + j * 1024) = acc[i][j];
    retry_st(stv, ws + OFF_ST, t * 16, T);
    WAITL(0);
    BAR();
    const float mean = __uint_as_float(stv.x), rstd = __builtin_amdgcn_rsqf(__uint_as_float(stv.z));
#pragma unroll
    for (int j = 0; j < 5; ++j) {
      f32x4 a = acc[0][j];
#pragma unroll
      for (int i = 1; i < 4; ++i) a += *(LAS f32x4*)(slotp((w - i) & 3, i - 1) + j * 1024);
      const int row = j * 16 + 4 * g + q;
      u32x2 z = trd(lds + L_Z + row * 128 + (((w * 2 + (p >> 1)) ^ (((row >> 1) & 3) << 1)) << 4) + (p & 1) * 8);
      float xs[4] = {bflo(xv[j].x), bfhi(xv[j].x), bflo(xv[j].y), bfhi(xv[j].y)};
      float zs[4] = {bflo(z.x), bfhi(z.x), bflo(z.y), bfhi(z.y)};
      float o[4];
#pragma unroll
      for (int r = 0; r < 4; ++r) o[r] = xs[r] + (xs[r] - mean) * rstd * zs[r] + a[r];
      u32x2 ov = {pk2(o[0], o[1]), pk2(o[2], o[3])};
      *(GAS u32x2*)((char*)out + (size_t)t * 1280 + (c2 * 80 + j * 16 + 4 * g) * 2) = ov;
    }
  } else {
    const int lw = wid - 4;
    u32 T;
    int ch;
    bool cv;
    if (isg) {
      u32 E;
      aload4_sc1(E, ws, FL_EPOCH);
      const char* xb = (const char*)x + (size_t)(rt * 256 + lw * 64) * 1280;
      const u32 xo0 = (lane >> 3) * 1280 + (((lane & 7) ^ (lane >> 4)) << 4);
      const u32 xo1 = (lane >> 3) * 1280 + (((lane & 7) ^ (4 | (lane >> 4))) << 4);
      const char* wb = (const char*)((lw >> 1) ? wu : wg) + ct * 128 + (lw & 1) * 32 * 3456;
      const u32 wo0 = (lane >> 3) * 3456 + (((lane & 7) ^ (((lane >> 4) & 1) << 1)) << 4);
      const u32 wo1 = (lane >> 3) * 3456 + (((lane & 7) ^ ((((lane >> 4) & 1) | 2) << 1)) << 4);
      auto issue = [&](int s) {
        const u32 lx = lb + (s % 3) * STG + lw * 8192;
        const u32 lwp = lb + (s % 3) * STG + 32768 + lw * 4096;
        const char* xs = xb + s * 128;
        const char* wsrc = wb + s * (64 * 3456);
#pragma unroll
        for (int i = 0; i < 4; ++i) glds(wsrc + i * (8 * 3456), (i & 1) ? wo1 : wo0, lwp + i * 1024);
#pragma unroll
        for (int i = 0; i < 8; ++i) glds(xs + i * 10240, (i & 1) ? xo1 : xo0, lx + i * 1024);
      };
 issue(0); issue(1); issue(2);
      const int rid = lw * 216 + gidx;
      const bool hasw = rid < 540;
      const int rks = rid / 10, rwq = rid % 10;
      if (hasw) {
        const u32 o = (lane >> 3) * 1280 + (((lane & 7) ^ ((lane >> 4) << 1)) << 4);
#pragma unroll
        for (int i = 0; i < 4; ++i) glds((const char*)wd + (size_t)(rks * 32 + i * 8) * 1280 + rwq * 128, o, lb + L_RT + lw * 4096 + i * 1024);
      }
      cv = lw < 2 || gidx < G2CH;
      ch = 40 * AUXCH + gidx + ((lw >= 2 && gidx < G2CH) ? 216 : 0);
      const int f = ((q >> 1) & 1) | ((g & 1) << 1);
      LAS char* wadr = lds + 32768 + (lw >> 1) * 8192 + (8 * g + q) * 128 + ((((lw & 1) * 4 + (p >> 1)) ^ (f << 1)) << 4) + (p & 1) * 8;
      LAS char* wadr1 = lds + 32768 + (lw >> 1) * 8192 + (8 * g + q) * 128 + (((((lw & 1) * 4 + (p >> 1)) ^ (f << 1)) << 4) ^ 32) + (p & 1) * 8;
      float c0 = 0.f, c1 = 0.f;
      const u32 ones = 0x3f803f80u;
      u32x2 wv[8];
      auto csld = [&](int s) {
        LAS char *w0 = wadr + (s % 3) * STG, *w1 = wadr1 + (s % 3) * STG;
        wv[0] = trd(w0); wv[1] = trd(w0 + 512); wv[2] = trd(w0 + 4096); wv[3] = trd(w0 + 4608);
        wv[4] = trd(w1); wv[5] = trd(w1 + 512); wv[6] = trd(w1 + 4096); wv[7] = trd(w1 + 4608);
      };
      auto csum = [&]() {
#pragma unroll
        for (int i = 0; i < 4; ++i) { c0 = dot2(wv[i].x, ones, c0); c0 = dot2(wv[i].y, ones, c0); c1 = dot2(wv[4 + i].x, ones, c1); c1 = dot2(wv[4 + i].y, ones, c1); }
        asm volatile("" : "+v"(c0), "+v"(c1));
      };
      if (hasw) WAITV(28); else WAITV(24);
      BAR();
      csld(0); csum();
      if (hasw) WAITV(16); else WAITV(12);
      BAR();
      csld(1); issue(3); csum();
      WAITV(12);
      BAR();
      csld(2); issue(4); csum();
      if (hasw) {
        const int row = 4 * g + q;
        u32x4 o[4];
#pragma unroll
        for (int dt = 0; dt < 4; ++dt) {
          LAS char* a = lds + L_RT + lw * 4096 + row * 128 + (((dt * 2 + (p >> 1)) ^ (((row >> 1) & 3) << 1)) << 4) + (p & 1) * 8;
          u32x2 lo = trd(a), hi = trd(a + 2048);
          o[dt] = u32x4{lo.x, lo.y, hi.x, hi.y};
        }
#pragma unroll
        for (int dt = 0; dt < 4; ++dt) {
          const int Dg = rwq * 4 + dt, cc = Dg / 5, jj = Dg % 5;
          st_sc1(rws, o[dt], OFF_WDT + ((cc * 54 + rks) * 5 + jj) * 1024 + lane * 16);
        }
        WAITV(16);
      } else WAITV(12);
      BAR();
      csld(3); issue(5); csum();
      asm volatile("s_waitcnt vmcnt(12)" : "+v"(E)::"memory");
      T = __builtin_amdgcn_readfirstlane(E) * 1664525u + 1013904223u;
      BAR();
      csld(4);
      if (lw == 3 && lane == 0) { *(LAS u32*)(lds + L_T) = T; stflag(ws, FL_A + b * 4, T); }
      issue(6);
      u32x4 kin[4], uin[2];
      u32 dd;
#pragma unroll
      for (int i = 0; i < 4; ++i) aload16(kin[i], (const char*)ssk + (size_t)ch * 4096 + i * 1024, lane * 16);
      aload2(dd, ssD + ch, 0);
      csum();
      if (lw == 3) WAITV(18); else WAITV(17);
      WAITL(0);
      BAR();
      csld(5); issue(7); csum();
      WAITV(17);
      BAR();
      csld(6); issue(8); csum();
      WAITV(12);
      BAR();
      csld(7);
      u32x4 fl, st;
      aload16_sc1(fl, ws + FL_A, lane * 16);
      aload16_sc1(st, ws + OFF_ST + (rt * 256 + lw * 64) * 16, lane * 16);
      issue(9); csum();
      asm volatile("s_waitcnt vmcnt(12)" : "+v"(fl), "+v"(st)::"memory");
      BAR();
      csld(8);
      if (!early_ok(fl, lane, T, 3)) wait_early(rws, lane, T, 3);
      retry_st(st, ws + OFF_ST + (rt * 256 + lw * 64) * 16, lane * 16, T);
#pragma unroll
      for (int i = 0; i < 2; ++i) aload16_sc1(uin[i], ws + OFF_UP + ch * 4096 + (2 * (lw & 1) + i) * 1024, lane * 16);
      wdma(0, sa0); wdma(1, sa0 + 5120);
      csum();
      {
        const float K1 = -2.0f * 0.7978845608028654f * 1.4426950408889634f, K2 = K1 * 0.044715f;
        const float v = __uint_as_float(st.z), rs = __builtin_amdgcn_rsqf(v);
        *(LAS f32x4*)(lds + L_ST + (lw * 64 + lane) * 16) = f32x4{-__uint_as_float(st.x), K1 * rs, K2 * rs * rs * rs, v};
      }
      WAITV(12);
      BAR();
      csld(9); csum();
      c0 += __shfl_xor(c0, 16); c0 += __shfl_xor(c0, 32);
      c1 += __shfl_xor(c1, 16); c1 += __shfl_xor(c1, 32);
      if (lane < 16) { *(LAS float*)(lds + L_CS + (lw * 32 + lane) * 4) = c0; *(LAS float*)(lds + L_CS + (lw * 32 + 16 + lane) * 4) = c1; }
      WAITL(0);
      BAR();
      asm volatile("s_waitcnt vmcnt(10)" : "+v"(kin[0]), "+v"(kin[1]), "+v"(kin[2]), "+v"(kin[3]), "+v"(uin[0]), "+v"(uin[1]), "+v"(dd)::"memory");
      conv_stage(lds + L_CV + lw * CVSZ, kin, dd, uin, lane);
      wdma(2, sa2); wdma(3, sa2 + 20480); wdma(4, sa4);
      if (lw < 2) wdma(5, sa5);
      if (!cv) {
        BAR();
        WAITV(0);
        if (lane == 0) *(volatile LAS u32*)(lds + L_WF + lw * 4) = 1u;
      }
    } else {
      T = aux_work(lds, ws, rws, x, kk, vv, shk, b, aux, auxr, rg, wid, lane);
      ch = aux * AUXCH + (wid >> 1);
      cv = true;
      u32x4 kin[4], uin[2];
      u32 dd;
      conv_ldk(kin, dd, ssk, ssD, ch, lane);
      wait_early(rws, lane, T, 2);
      conv_ldu(uin, rws, ch, lw & 1, lane);
      conv_stage(lds + L_CV + lw * CVSZ, kin, dd, uin, lane);
    }
#pragma unroll 1
    for (int round = isg ? 1 : 0; round < 2; ++round) {
      if (cv) {
        LAS char* cb = lds + L_CV + lw * CVSZ;
        u32x4 kin[4], uin[2];
        u32 dd;
        const int chn = round == 0 ? ch + 2 : ch;
        conv_run(lds, cb, lds + L_CV + (lw ^ 1) * CVSZ, wid, wid ^ 1, (u32)round + 1u, ws, rws, ch, lw & 1, lane, T, wid,
                 [&] { if (isg) BAR(); },
                 [&] {
                   if (isg) { WAITV(0); if (lane == 0) *(volatile LAS u32*)(lds + L_WF + lw * 4) = 1u; }
                   if (round == 0) { conv_ldk(kin, dd, ssk, ssD, chn, lane); conv_ldu(uin, rws, chn, lw & 1, lane); }
                 });
        ch = chn;
        if (round == 0) { BAR(); conv_stage(cb, kin, dd, uin, lane); }
      }
    }
    if (lw == 3) {
      const u32 zf = FL_Z + ((rq >> 4) * 1024 + c2 * 80) * 4;
#pragma unroll 1
      for (int spins = 0; spins < SPIN_MAX; ++spins) {
        const u32 v0 = ldflag(ws, zf + lane * 4), v1 = ldflag(ws, zf + (64 + (lane & 15)) * 4);
        if (__all(v0 == T && v1 == T)) break;
        __builtin_amdgcn_s_sleep(4);
      }
      asm volatile("" ::: "memory");
      u32x4 zv[10];
#pragma unroll
      for (int i = 0; i < 10; ++i) zv[i] = ld_sc1(rws, (lane >> 3) * 4096 + (lane & 7) * 16, OFF_Z + (c2 * 80 + i * 8) * 4096 + rq * 128);
#pragma unroll
      for (int i = 0; i < 10; ++i) { const int row = i * 8 + (lane >> 3); *(LAS u32x4*)(lds + L_Z + row * 128 + (((lane & 7) ^ (((row >> 1) & 3) << 1)) << 4)) = zv[i]; }
    }
    WAITL(0);
    BAR();
    if (lw == 3 && b == 0) {
      wait_early(rws, lane, T, 3);
      if (lane == 0) stflag(ws, FL_EPOCH, T);
    }
  }
}

extern "C" void kernel_launch(void* const* d_in, const int* in_sizes, int n_in, void* d_out, int out_size, void* d_ws, size_t ws_size,
                              hipStream_t stream) {
  kf<<<256, 512, 0, stream>>>((const u16*)d_in[0], (const u16*)d_in[1], (const u16*)d_in[2], (const u16*)d_in[3], (const u16*)d_in[4],
                              (const u16*)d_in[5], (const u16*)d_in[7], (const u16*)d_in[8], (const u16*)d_in[9], (char*)d_ws, (u16*)d_out);
}
```

```cpp
#include <hip/hip_runtime.h>
#include <hip/hip_bf16.h>

typedef unsigned short u16;
typedef unsigned int u32;
typedef unsigned long long u64;
typedef __attribute__((ext_vector_type(8))) __bf16 bf16x8;
typedef __attribute__((ext_vector_type(2))) __bf16 bf16x2;
typedef __attribute__((ext_vector_type(4))) short s16x4;
typedef __attribute__((ext_vector_type(4))) float f32x4;
typedef __attribute__((ext_vector_type(2))) float f32x2;
typedef __attribute__((ext_vector_type(4))) u32 u32x4;
typedef __attribute__((ext_vector_type(2))) u32 u32x2;
typedef __amdgpu_buffer_rsrc_t rsrc_t;
#define GAS __attribute__((address_space(1)))
#define LAS __attribute__((address_space(3)))
#define CAS __attribute__((address_space(4)))
#define DI __device__ __forceinline__
#define BAR() asm volatile("s_barrier" ::: "memory")
#define WAITV(n) asm volatile("s_waitcnt vmcnt(" #n ")" ::: "memory")
#define WAITL(n) asm volatile("s_waitcnt lgkmcnt(" #n ")" ::: "memory")
#define SCHED() __builtin_amdgcn_sched_barrier(0)
#define RLX_AGENT __ATOMIC_RELAXED, __HIP_MEMORY_SCOPE_AGENT

constexpr int S = 2048, D = 640, F = 1728;
constexpr u32 OFF_HT = 0;
constexpr u32 OFF_WDT = 8u << 20;
constexpr u32 OFF_UP = 11u << 20;
constexpr u32 OFF_Z = 14u << 20;
constexpr u32 OFF_ST = 17u << 20;
constexpr u32 OFF_FL = (17u << 20) + 65536;
constexpr u32 FL_EPOCH = OFF_FL;
constexpr u32 FL_ACK = OFF_FL + 1024;
constexpr u32 FL_U = OFF_FL + 4096;
constexpr u32 FL_A = OFF_FL + 32768;
constexpr u32 FL_H = OFF_FL + 65536;
constexpr u32 FL_Z = FL_H + 131072;
constexpr u32 WS_USED = FL_Z + 131072;
constexpr int SPIN_MAX = 1 << 18;
#define CW 4
#define CSL 1
#define SP 1
#define ZK2 9
#define ZK 6
#define GF 6
#define WIN 8
#define WIN2 WIN
#define THR(n) asm volatile("s_waitcnt vmcnt(%0)" ::"n"(n) : "memory")
#define NP8 0
#define WSW 20
#define A2PRE 6
#define SROW srow
constexpr int G2CH = 400 - 216;


DI int lane_id() { int l; asm volatile("v_mbcnt_lo_u32_b32 %0, -1, 0\n\tv_mbcnt_hi_u32_b32 %0, -1, %0" : "=v"(l)); return l; }
DI float bflo(u32 v) { return __uint_as_float(v << 16); }
DI float bfhi(u32 v) { return __uint_as_float(v & 0xffff0000u); }
DI u32 pk2(float a, float b) { f32x2 v = {a, b}; bf16x2 r = __builtin_convertvector(v, bf16x2); return __builtin_bit_cast(u32, r); }
DI u32 pk2v(f32x2 v) { bf16x2 r = __builtin_convertvector(v, bf16x2); return __builtin_bit_cast(u32, r); }
DI f32x4 mfma(bf16x8 a, bf16x8 b, f32x4 c) { return __builtin_amdgcn_mfma_f32_16x16x32_bf16(a, b, c, 0, 0, 0); }
DI u32x2 trd(LAS char* p) { s16x4 v = __builtin_amdgcn_ds_read_tr16_b64_v4i16((LAS s16x4*)p); return __builtin_bit_cast(u32x2, v); }
DI bf16x8 cat(u32x2 a, u32x2 b) { u32x4 v = {a.x, a.y, b.x, b.y}; return __builtin_bit_cast(bf16x8, v); }
DI float dot2(u32 a, u32 b, float c) { return __builtin_amdgcn_fdot2_f32_bf16(__builtin_bit_cast(bf16x2, a), __builtin_bit_cast(bf16x2, b), c, false); }
template <int C> DI float dpp_add(float v) { return v + __int_as_float(__builtin_amdgcn_update_dpp(0, __float_as_int(v), C, 0xf, 0xf, true)); }
DI float rsum16(float v) { return dpp_add<0x128>(dpp_add<0x124>(dpp_add<0x122>(dpp_add<0x121>(v)))); }
DI rsrc_t mkrsrc(const void* p, u32 n) { return __builtin_amdgcn_make_buffer_rsrc((void*)p, 0, n, 0x00020000); }
DI u32x4 ld_sc1(rsrc_t r, u32 vo, u32 so) { return __builtin_amdgcn_raw_buffer_load_b128(r, vo, so, 16); }
DI void st_sc1(rsrc_t r, u32x4 v, u32 vo) { __builtin_amdgcn_raw_buffer_store_b128(v, r, vo, 0, 16); }
DI u32 ldflag(char* ws, u32 off) { return __hip_atomic_load((GAS u32*)(ws + off), RLX_AGENT); }
DI void stflag(char* ws, u32 off, u32 v) { __hip_atomic_store((GAS u32*)(ws + off), v, RLX_AGENT); }
DI void glds(const void* sbase, u32 voff, u32 la) {
  asm volatile("s_mov_b32 m0, %2\n\ts_nop 0\n\tglobal_load_lds_dwordx4 %0, %1" ::"v"(voff), "s"(sbase), "s"(la) : "memory");
}
DI void glds_sc1(const void* sbase, u32 voff, u32 la) {
  asm volatile("s_mov_b32 m0, %2\n\ts_nop 0\n\tglobal_load_lds_dwordx4 %0, %1 sc1" ::"v"(voff), "s"(sbase), "s"(la) : "memory");
}
DI void aload16_sc1(u32x4& v, const void* sbase, u32 voff) { asm volatile("global_load_dwordx4 %0, %1, %2 sc1" : "=v"(v) : "v"(voff), "s"(sbase) : "memory"); }
DI void aload16(u32x4& v, const void* sbase, u32 voff) { asm volatile("global_load_dwordx4 %0, %1, %2" : "=v"(v) : "v"(voff), "s"(sbase) : "memory"); }
DI void aload4_sc1(u32& v, const void* sbase, u32 voff) { asm volatile("global_load_dword %0, %1, %2 sc1" : "=v"(v) : "v"(voff), "s"(sbase) : "memory"); }
DI u32 get_tag(const char* ws) {
  u32 e;
  asm volatile("s_load_dword %0, %1, 0x0\n\ts_waitcnt lgkmcnt(0)" : "=s"(e) : "s"(ws + FL_EPOCH) : "memory");
  return e * 1664525u + 1013904223u;
}
template <int SL>
DI void wait_flags(char* ws, u32 off, u32 T) {
#pragma unroll 1
  for (int spins = 0; spins < SPIN_MAX; ++spins) {
    const u32 v = ldflag(ws, off);
    if (__all(v == T)) break;
    __builtin_amdgcn_s_sleep(SL);
  }
  asm volatile("" ::: "memory");
}
DI void retry4(u32& v, const char* sbase, u32 vo, u32 T) {
#pragma unroll 1
  for (int spins = 0; spins < SPIN_MAX && !__all(v == T); ++spins)
    asm volatile("s_sleep 2\n\tglobal_load_dword %0, %1, %2 sc1\n\ts_waitcnt vmcnt(0)" : "=v"(v) : "v"(vo), "s"(sbase) : "memory");
}
DI void retry16(u32x4& v, const char* sbase, u32 vo, u32 T) {
#pragma unroll 1
  for (int spins = 0; spins < SPIN_MAX && !__all(v.x == T && v.y == T && v.z == T && v.w == T); ++spins)
    asm volatile("global_load_dwordx4 %0, %1, %2 sc1\n\ts_waitcnt vmcnt(0)" : "=v"(v) : "v"(vo), "s"(sbase) : "memory");
}
DI void retry_st(u32x4& st, const char* sbase, u32 vo, u32 T) {
#pragma unroll 1
  for (int spins = 0; spins < SPIN_MAX && !__all(st.y == T && st.w == T); ++spins)
    asm volatile("global_load_dwordx4 %0, %1, %2 sc1\n\ts_waitcnt vmcnt(0)" : "=v"(st) : "v"(vo), "s"(sbase) : "memory");
}
DI void wait_lds4(LAS char* p) {
#pragma unroll 1
  for (int spins = 0; spins < (SPIN_MAX << 6); ++spins) {
    asm volatile("" ::: "memory");
    const u32x4 v = *(LAS u32x4*)p;
    if (__builtin_amdgcn_readfirstlane(v.x & v.y & v.z & v.w)) break;
    __builtin_amdgcn_s_sleep(1);
  }
  asm volatile("" ::: "memory");
}
DI void wait_lds2(LAS char* p) {
#pragma unroll 1
  for (int spins = 0; spins < (SPIN_MAX << 6); ++spins) {
    asm volatile("" ::: "memory");
    const u32x2 v = *(LAS u32x2*)p;
    if (__builtin_amdgcn_readfirstlane(v.x & v.y)) break;
    __builtin_amdgcn_s_sleep(1);
  }
  asm volatile("" ::: "memory");
}
DI void wait_lds1(LAS char* p) {
#pragma unroll 1
  for (int spins = 0; spins < (SPIN_MAX << 6); ++spins) {
    asm volatile("" ::: "memory");
    const u32 v = *(LAS u32*)p;
    if (__builtin_amdgcn_readfirstlane(v)) break;
    __builtin_amdgcn_s_sleep(2);
  }
  asm volatile("" ::: "memory");
}
DI void wait_cnt(LAS char* p, u32 n) {
#pragma unroll 1
  for (int spins = 0; spins < (SPIN_MAX << 6); ++spins) {
    asm volatile("" ::: "memory");
    const u32 v = *(LAS u32*)p;
    if ((u32)__builtin_amdgcn_readfirstlane(v) >= n) break;
    __builtin_amdgcn_s_sleep(CSL);
  }
  asm volatile("" ::: "memory");
}
DI void set_lds(LAS char* p, int lane) {
  asm volatile("" ::: "memory");
  if (lane == 0) *(volatile LAS u32*)p = 1u;
}

constexpr int STG = 49152;
constexpr int L_U = 3 * STG;
constexpr int L_ST = L_U + 10240;
constexpr int L_CS = L_ST + 4096;
constexpr int L_FL = L_CS + 512;
constexpr int L_CF = L_FL, L_KF = L_FL + 16, L_P1 = L_FL + 32, L_P2 = L_FL + 48, L_SD = L_FL + 64, L_TF = L_FL + 96, L_GR = L_FL + 128;
constexpr int L_T = L_FL + 512;
constexpr int LDS_SZ = L_T + 16;
constexpr int L_RT = L_U;
constexpr int L_K = STG, KSZ = 4224;
constexpr int L_SCR = 65536;
constexpr int OPS = 9216;

DI void conv_pads(LAS char* kb, LAS char* ub, int lane) {
  if (lane < 16) *(LAS u32*)(kb + lane * 4) = 0u;
  *(LAS u32x2*)(ub + lane * 8) = u32x2{0u, 0u};
}
DI void conv_patch(LAS char* kb, u32 dd, int lane) {
  if (lane == 0) { const u32 k0 = *(LAS u16*)(kb + 64); *(LAS u16*)(kb + 64) = (u16)pk2(bflo(k0) + bflo(dd), 0.f); }
}

template <int HH, class LDX>
DI void conv_rounds(LAS char* lds, u32 lb, int wid, int nr, int ch0, u32 pf, char* ws, rsrc_t rws, int lane, u32 T, LDX ldx) {
  const int g = lane >> 4, q = (lane >> 2) & 3, p = lane & 3, tl = lane & 15, pr = wid >> 1;
  constexpr int NA = HH ? 12 : 16, EMAX = HH ? 10 : 14, NB = (EMAX + 2) * 4, PF = 6;
  constexpr int TN[4] = {HH ? 2 : 0, HH ? 3 : 1, HH ? 4 : 6, HH ? 5 : 7};
  wait_lds2(lds + (nr == 1 ? L_KF : L_TF) + pr * 8);
  LAS char* const al = lds + 64 + (32 * g + 16 - 4 * q + 4 * p) * 2;
  bf16x8 A[NA];
  {
    LAS char* a = al + (nr == 1 ? L_K + pr * KSZ : pr * OPS);
#pragma unroll
    for (int s = NA - 1; s >= 0; --s) A[s] = cat(trd(a + s * 256), trd(a + s * 256 - 32));
  }
  if (nr != 1) {
    SCHED();
    const u32 hb = (wid & 1) * 2048;
    wait_flags<4>(ws, FL_U + (((ch0 - pr + (lane >> 5) * 5) >> 6) * 16 + (((ch0 - pr) / 6) & 15)) * 128 + (lane & 31) * 4, T);
#pragma unroll
    for (int r = 0; r < 3; ++r) {
      const u32 ub2 = r == 2 ? L_U + pr * 5120 : (r * 2 + pr) * OPS + KSZ;
#pragma unroll
      for (int i = 0; i < 2; ++i) glds_sc1(ws + OFF_UP + (ch0 + r * 2) * 4096 + hb + i * 1024, lane * 16, lb + ub2 + 512 + hb + i * 1024);
    }
    WAITV(4);
    set_lds(lds + L_KF + wid * 4, lane);
    wait_lds2(lds + L_KF + pr * 8);
  }
#pragma unroll 1
  for (int rr = 0; rr < nr; ++rr) {
    const bool last = rr == nr - 1;
    LAS char* kb = lds + (last ? L_K + pr * KSZ : (rr * 2 + pr) * OPS);
    LAS char* ub = last ? lds + L_U + pr * 5120 : kb + KSZ;
    const int ch = ch0 + rr * 2;
    LAS char* an = al + (rr + 2 >= nr ? L_K + pr * KSZ : (rr * 2 + 2 + pr) * OPS);
    LAS char* ba[2][4];
#pragma unroll
    for (int pc = 0; pc < 2; ++pc) {
      const int i = 4 * tl - 8 * g - 4 + 4 * pc, L = i >> 4;
#pragma unroll
      for (int rho = 0; rho < 4; ++rho) ba[pc][rho] = ub + 256 + L * 128 + (i & 15) * 2 + ((rho + (L >> 1)) & 3) * 32;
    }
    bf16x8 B[NB];
    auto ldb = [&](int f) {
      const int e1 = f >> 2, rho = (f + e1 + 3) & 3;
      B[f] = cat(*(LAS u32x2*)(ba[0][rho] + e1 * 256), *(LAS u32x2*)(ba[1][rho] + e1 * 256));
    };
#pragma unroll
    for (int f = 0; f < PF; ++f) ldb(f);
    SCHED();
    f32x4 acc[4][4];
#pragma unroll
    for (int f = 0; f < NB; ++f) {
      const int e = (f >> 2) - 1, r = f & 3;
      if (f + PF < NB) ldb(f + PF);
#pragma unroll
      for (int n = 0; n < 4; ++n)
        if (2 * TN[n] - e >= 0) acc[r][n] = mfma(A[2 * TN[n] - e], B[f], e < 0 ? f32x4{0.f, 0.f, 0.f, 0.f} : acc[r][n]);
      if (r == 3) A[EMAX - e] = cat(trd(an + (EMAX - e) * 256), trd(an + (EMAX - e) * 256 - 32));
      SCHED();
      if (f == 3 && last) set_lds(lds + L_CF + wid * 4, lane);
      if (f == GF) {
        WAITV(0);
        if (pf) stflag(ws, pf, T);
        if (last) ldx();
        SCHED();
      }
    }
    const int lam = 4 * tl + g;
    const int sl = (lam + 63) & 63;
    const int srcl = (16 * (sl & 3) + (sl >> 2)) * 4;
    float cin[4][3];
    constexpr int SND = HH ? 3 : 1, RCV = HH ? 0 : 2;
#pragma unroll
    for (int n = 0; n < 4; ++n) {
      const float c0 = acc[1][n][3] + acc[2][n][2] + acc[3][n][1], c1 = acc[2][n][3] + acc[3][n][2], c2 = acc[3][n][3];
      if (n == SND && lane == 63) *(volatile LAS u32x4*)(lds + L_GR + rr * 64 + wid * 16) = u32x4{__float_as_uint(c0), __float_as_uint(c1), __float_as_uint(c2), 1u};
      cin[n][0] = __int_as_float(__builtin_amdgcn_ds_bpermute(srcl, __float_as_int(c0)));
      cin[n][1] = __int_as_float(__builtin_amdgcn_ds_bpermute(srcl, __float_as_int(c1)));
      cin[n][2] = __int_as_float(__builtin_amdgcn_ds_bpermute(srcl, __float_as_int(c2)));
    }
    u32x4 pc;
#pragma unroll 1
    for (int spins = 0; spins < (SPIN_MAX << 6); ++spins) {
      pc = *(volatile LAS u32x4*)(lds + L_GR + rr * 64 + (wid ^ 1) * 16);
      if (__builtin_amdgcn_readfirstlane(pc.w)) break;
    }
    u32 y[4][2];
#pragma unroll
    for (int n = 0; n < 4; ++n) {
      float c[3];
#pragma unroll
      for (int k = 0; k < 3; ++k) {
        const float alt = n == RCV ? __uint_as_float(pc[k]) : (n == 0 ? 0.f : cin[n ? n - 1 : 0][k]);
        c[k] = lane == 0 ? alt : cin[n][k];
      }
      const f32x4 d0 = acc[0][n], d1 = acc[1][n], d2 = acc[2][n], d3 = acc[3][n];
      y[n][0] = pk2(d0[0] + c[0], d0[1] + d1[0] + c[1]);
      y[n][1] = pk2(d0[2] + d1[1] + d2[0] + c[2], d0[3] + d1[2] + d2[1] + d3[0]);
    }
#pragma unroll
    for (int n = 0; n < 4; n += 2) {
      const auto s0 = __builtin_amdgcn_permlane16_swap(y[n][0], y[n + 1][0], false, false);
      const auto s1 = __builtin_amdgcn_permlane16_swap(y[n][1], y[n + 1][1], false, false);
      st_sc1(rws, u32x4{s0[0], s1[0], s0[1], s1[1]}, OFF_Z + ch * 4096 + (TN[n] + (g & 1)) * 512 + (4 * tl + (g & 2)) * 8);
    }
    {
      const int cz = ch / 80, row = ch - cz * 80, hi = row >= 40, r1 = row - hi * 40, od = r1 >= 24;
      const int rqz = HH ? 8 + lane : (lane < 8 ? lane : lane + 16);
      pf = lane < 16 ? FL_Z + (rqz * 8 + cz) * 512 + (hi * 2 + od) * 128 + (r1 - od * 24) * 4 : 0u;
    }
  }
  __builtin_amdgcn_s_waitcnt(0x0F70);
  WAITV(0);
  if (pf) stflag(ws, pf, T);
}

DI void row_stats1(const u32x4 (&xr)[5], float& mean, f32x2& s2) {
  float s1 = 0.f;
#pragma unroll
  for (int i = 0; i < 5; ++i)
#pragma unroll
    for (int j = 0; j < 4; ++j) s1 = dot2(xr[i][j], 0x3f803f80u, s1);
  s1 = rsum16(s1);
  mean = s1 * (1.f / D);
  s2 = f32x2{0.f, 0.f};
  const f32x2 m2 = {mean, mean};
#pragma unroll
  for (int i = 0; i < 2; ++i)
#pragma unroll
    for (int j = 0; j < 4; ++j) { const f32x2 a = f32x2{bflo(xr[i][j]), bfhi(xr[i][j])} - m2; s2 = __builtin_elementwise_fma(a, a, s2); }
  asm volatile("" : "+v"(mean), "+v"(s2));
}
DI void row_stats2(const u32x4 (&xr)[5], float mean, f32x2 s2, rsrc_t r, int row, int tl, u32 T) {
  const f32x2 m2 = {mean, mean};
#pragma unroll
  for (int i = 2; i < 5; ++i)
#pragma unroll
    for (int j = 0; j < 4; ++j) { const f32x2 a = f32x2{bflo(xr[i][j]), bfhi(xr[i][j])} - m2; s2 = __builtin_elementwise_fma(a, a, s2); }
  const float var = rsum16(s2.x + s2.y) * (1.f / D) + 1e-5f;
  const u32x4 st = {__float_as_uint(mean), T, __float_as_uint(var), T};
  if (tl == 0) st_sc1(r, st, OFF_ST + row * 16);
}

DI void aux_work(LAS char* lds, u32 lb, char* ws, rsrc_t rws, const u16* kk, const u16* vv, const u16* shk, const u16* ssk, const u16* ssD,
                 int aux, int wid, int lane, u32& T) {
  const int c = lane & 7, rgp = lane >> 3, w4 = wid & 3, pr = w4 >> 1;
  const int un = aux * 4 + w4, cq = (un >> 5) + (wid >> 2) * 5, tq = un & 31;
  const int t0 = tq * 64 + rgp * 8;
  if (wid == 0) *(LAS u32x2*)(lds + L_FL + lane * 8) = u32x2{0u, 0u};
  WAITL(0);
  BAR();
  u32x4 kr[11], vr[8], sk[4];
  if (wid < 4) __builtin_amdgcn_s_setprio(2);
  const size_t cofs = (size_t)(cq * 64 + c * 8) * 2;
#pragma unroll
  for (int i = 0; i < 4; ++i) sk[i] = *(const GAS u32x4*)((const char*)shk + cofs * 4 + i * 16);
#pragma unroll
  for (int i = 0; i < 11; ++i) {
    const int t = t0 - 3 + i;
    const int tc = t < 0 ? 0 : t;
    u32x4 v = *(const GAS u32x4*)((const char*)kk + (size_t)tc * 1280 + cofs);
    if (t < 0) v = u32x4{0, 0, 0, 0};
    kr[i] = v;
  }
#pragma unroll
  for (int i = 0; i < 8; ++i) vr[i] = *(const GAS u32x4*)((const char*)vv + (size_t)(t0 + i) * 1280 + cofs);
  SCHED();
  T = get_tag(ws);
  if (wid < 4 && !(wid & 1)) {
    conv_pads(lds + pr * OPS, lds + pr * OPS + KSZ, lane);
    conv_pads(lds + (2 + pr) * OPS, lds + (2 + pr) * OPS + KSZ, lane);
    conv_pads(lds + L_K + pr * KSZ, lds + L_U + pr * 5120, lane);
  }
  SCHED();
  LAS char* ub = lds + L_SCR + wid * 8192;
  u32 wa[4];
#pragma unroll
  for (int r = 0; r < 4; ++r) wa[r] = c * 1024 + ((((r + (tq >> 1)) & 3) * 8 + rgp + 4 * c) & 31) * 4;
#pragma unroll
  for (int jj = 0; jj < 4; ++jj) {
    f32x2 kf[11], tap[4];
#pragma unroll
    for (int i = 0; i < 11; ++i) kf[i] = f32x2{bflo(kr[i][jj]), bfhi(kr[i][jj])};
    tap[0] = f32x2{bflo(sk[jj][0]), bflo(sk[jj][2])}; tap[1] = f32x2{bfhi(sk[jj][0]), bfhi(sk[jj][2])};
    tap[2] = f32x2{bflo(sk[jj][1]), bflo(sk[jj][3])}; tap[3] = f32x2{bfhi(sk[jj][1]), bfhi(sk[jj][3])};
    f32x2 u[8];
#pragma unroll
    for (int i = 0; i < 8; ++i) {
      f32x2 a = kf[i] * tap[3];
      a = __builtin_elementwise_fma(kf[i + 1], tap[2], a); a = __builtin_elementwise_fma(kf[i + 2], tap[1], a); a = __builtin_elementwise_fma(kf[i + 3], tap[0], a);
      u[i] = a * f32x2{bflo(vr[i][jj]), bfhi(vr[i][jj])};
    }
#pragma unroll
    for (int hh = 0; hh < 2; ++hh)
#pragma unroll
      for (int r = 0; r < 4; ++r) *(LAS u32*)(ub + wa[r] + (jj * 2 + hh) * 128) = pk2(u[r][hh], u[r + 4][hh]);
    SCHED();
  }
#pragma unroll
  for (int it = 0; it < 8; ++it) {
    const u32x4 v = *(LAS u32x4*)(ub + (it * 8 + rgp) * 128 + (((c + it) & 7) << 4));
    st_sc1(rws, v, OFF_UP + (cq * 64 + it * 8 + rgp) * 4096 + tq * 128 + c * 16);
  }
  SCHED();
  set_lds(lds + L_SD + wid * 4, lane);
  const u32 ufl = lane < 16 ? FL_U + (cq * 16 + lane) * 128 + tq * 4 : FL_ACK + (216 + cq * 32 + tq) * 4;
  if (wid >= 4) {
    WAITV(0);
    if (lane < 17) stflag(ws, ufl, T);
  } else {
    const int ch = aux * 6 + pr;
    const u32 hb = (wid & 1) * 2048;
    u32 dd[3];
#pragma unroll
    for (int r = 0; r < 3; ++r) {
      dd[r] = (*(const CAS u32*)(uintptr_t)(ssD + aux * 6 + r * 2) >> (pr * 16)) & 0xffffu;
      const u32 kb = r == 2 ? L_K + pr * KSZ : (r * 2 + pr) * OPS;
#pragma unroll
      for (int i = 0; i < 2; ++i) glds((const char*)ssk + (size_t)(ch + r * 2) * 4096 + hb + i * 1024, lane * 16, lb + kb + 64 + hb + i * 1024);
    }
    WAITV(6);
    if (lane < 17) stflag(ws, ufl, T);
    WAITV(0);
    if (!(wid & 1)) {
#pragma unroll
      for (int r = 0; r < 3; ++r) conv_patch(lds + (r == 2 ? L_K + pr * KSZ : (r * 2 + pr) * OPS), dd[r], lane);
    }
    WAITL(0);
    set_lds(lds + L_TF + wid * 4, lane);
  }
}

__global__ __launch_bounds__(512, 2) void kf(const u16* __restrict__ x, const u16* __restrict__ kk, const u16* __restrict__ vv,
                                             const u16* __restrict__ shk, const u16* __restrict__ ssk, const u16* __restrict__ ssD,
                                             const u16* __restrict__ wg, const u16* __restrict__ wu, const u16* __restrict__ wd,
                                             char* __restrict__ ws, u16* __restrict__ out) {
  __shared__ __attribute__((aligned(1024))) char lds_[LDS_SZ];
  LAS char* lds = (LAS char*)lds_;
  const u32 lb = (u32)(uintptr_t)lds;
  if ((blockIdx.x >> 3) < CW) {
    u64 pc; u32 e1, e2;
    asm volatile("s_getpc_b64 %0\n.Lkf_h%=:\n\ts_mov_b32 %1, .Lkf_e-.Lkf_h%=-4\n\ts_mov_b32 %2, .Lkf_f-.Lkf_h%=-4" : "=s"(pc), "=s"(e1), "=s"(e2));
    const u32 o = min((threadIdx.x + (blockIdx.x & 8) * 64) * 64, max(e1, e2));
    asm volatile("s_mov_b32 m0, %2\n\ts_nop 0\n\tglobal_load_lds_dword %0, %1" ::"v"(o), "s"(pc), "s"(lb + L_ST + 2048) : "memory");
  }
  const int b = blockIdx.x, xcd = b & 7, slot = b >> 3;
  const int cg = xcd & 3, rg = xcd >> 2;
  const int ncol = cg == 3 ? 6 : 7;
  const int wid = __builtin_amdgcn_readfirstlane(threadIdx.x >> 6), lane = threadIdx.x & 63;
  const bool isg = slot < 4 * ncol;
  const int rt = rg * 4 + (slot & 3), ct = cg * 7 + (slot >> 2);
  const int w4 = wid & 3;
  u32 xo0 = (lane >> 3) * 1280 + (((lane & 7) ^ (lane >> 4)) << 4);
  u32 xo1 = (lane >> 3) * 1280 + (((lane & 7) ^ (4 | (lane >> 4))) << 4);
  u32 wo0 = (lane >> 3) * 3456 + (((lane & 7) ^ (((lane >> 4) & 1) << 1)) << 4);
  u32 wo1 = (lane >> 3) * 3456 + (((lane & 7) ^ ((((lane >> 4) & 1) | 2) << 1)) << 4);
  u32 xoff = (rt * 256 + w4 * 64) * 1280, woff = ct * 128 + (w4 & 1) * 32 * 3456, lxb = lb + w4 * 8192, lwb = lb + 32768 + w4 * 4096;
  int hg = (slot - 4 * ncol) & (3 - wid);
  asm volatile("" : "+v"(xo0), "+v"(xo1), "+v"(wo0), "+v"(wo1), "+s"(xoff), "+s"(woff), "+s"(lxb), "+s"(lwb), "+s"(hg));
  asm volatile("" : "+s"(x), "+s"(ws), "+s"(wg), "+s"(wu), "+s"(kk), "+s"(vv), "+s"(shk));
  const char* xb = (const char*)x + xoff;
  const char* wb = (const char*)((w4 >> 1) ? wu : wg) + woff;
  auto issue = [&](int s) {
    const u32 lx = lxb + (s % 3) * STG, lwp = lwb + (s % 3) * STG;
    const char* xs = xb + s * 128;
    const char* wsrc = wb + s * (64 * 3456);
#pragma unroll
    for (int i = 0; i < 4; ++i) glds(wsrc + i * (8 * 3456), (i & 1) ? wo1 : wo0, lwp + i * 1024);
#pragma unroll
    for (int i = 0; i < 8; ++i) glds(xs + i * 10240, (i & 1) ? xo1 : xo0, lx + i * 1024);
  };
  if (hg < 0) {
    __builtin_amdgcn_s_setprio(3);
 issue(0); issue(1);
  }
  asm volatile("" ::: "memory");
  const int g = lane >> 4, q = (lane >> 2) & 3, p = lane & 3, tl = lane & 15;
  const int auxr = cg < 3 ? cg * 4 + (slot - 28) : 12 + (slot - 24);
  const int aux = rg * 20 + auxr;
  const int gidx = rt * 27 + ct;
  const int rq = xcd * 4 + (slot & 3), c2 = slot >> 2;
  const rsrc_t rws = mkrsrc(ws, WS_USED);
  const int pr = w4 >> 1;
  u32 T;
  const bool cv = !isg || pr == 0 || gidx < G2CH;
  const int chl = isg ? 240 + gidx + (pr && cv ? 216 : 0) : aux * 6 + pr;
  if (!isg) aux_work(lds, lb, ws, rws, kk, vv, shk, ssk, ssD, aux, wid, lane, T);

  if (wid < 4) {
    u32 hfl = 0;
    if (isg) {
      if (wid == 0) *(LAS u32x2*)(lds + L_FL + lane * 8) = u32x2{0u, 0u};
      const int wr = wid >> 1, wc = wid & 1;
      u32 ax0 = (wr * 128 + tl) * 128 + ((g ^ (tl >> 1)) << 4);
      u32 ax1 = ax0 ^ 64;
      const int f = ((q >> 1) & 1) | ((g & 1) << 1);
      u32 aw0 = 32768 + (8 * g + q) * 128 + (((wc * 4 + (p >> 1)) ^ (f << 1)) << 4) + (p & 1) * 8;
      u32 aw1 = aw0 ^ 32;
      f32x4 acc[8][4];
#pragma unroll
      for (int i = 0; i < 8; ++i)
#pragma unroll
        for (int j = 0; j < 4; ++j) { acc[i][j] = f32x4{0.f, 0.f, 0.f, 0.f}; asm volatile("" : "+v"(acc[i][j])); }
      asm volatile("" : "+v"(ax0), "+v"(ax1), "+v"(aw0), "+v"(aw1));
      f32x4 cs[4];
      {
        bf16x8 X0[8], W0[4], X1[8], W1[4];
        auto ldf = [&](bf16x8(&X)[8], bf16x8(&W)[4], u32 ax, u32 wa, u32 wb2, int ko) {
          W[0] = cat(trd(lds + wa + ko), trd(lds + wa + ko + 512));
          X[0] = *(LAS bf16x8*)(lds + ax);
          W[1] = cat(trd(lds + wb2 + ko), trd(lds + wb2 + ko + 512));
          W[2] = cat(trd(lds + wa + ko + 8192), trd(lds + wa + ko + 8192 + 512));
          W[3] = cat(trd(lds + wb2 + ko + 8192), trd(lds + wb2 + ko + 8192 + 512));
#pragma unroll
          for (int i = 1; i < 8; ++i) X[i] = *(LAS bf16x8*)(lds + ax + i * 2048);
        };
        auto mma = [&](bf16x8(&X)[8], bf16x8(&W)[4]) {
#pragma unroll
          for (int i = 0; i < 8; ++i)
#pragma unroll
            for (int j = 0; j < 4; ++j) acc[i][j] = mfma(W[j], X[i], acc[i][j]);
        };
        auto pattern = [&]() {
#pragma unroll
          for (int i = 0; i < 16; ++i) { __builtin_amdgcn_sched_group_barrier(0x008, 1, 0); __builtin_amdgcn_sched_group_barrier(0x100, 1, 0); }
          __builtin_amdgcn_sched_group_barrier(0x008, 16, 0);
        };
        WAITL(0);
        SCHED();
        BAR();
        ldf(X0, W0, ax0, aw0, aw1, 0);
        SCHED();
        int buf = 0;
#pragma unroll 1
        for (int s = 0; s < 9; ++s) {
          ldf(X1, W1, ax1, aw0, aw1, 4096);
          mma(X0, W0);
          pattern();
          SCHED();
          asm volatile("s_waitcnt vmcnt(0) lgkmcnt(0)" ::: "memory");
          BAR();
          const int dlt = buf == 2 ? -2 * STG : STG;
          buf = buf == 2 ? 0 : buf + 1;
          ax0 += dlt; ax1 += dlt; aw0 += dlt; aw1 += dlt;
          SCHED();
          ldf(X0, W0, ax0, aw0, aw1, 0);
          mma(X1, W1);
          pattern();
          SCHED();
        }
        ldf(X1, W1, ax1, aw0, aw1, 4096);
        mma(X0, W0);
        pattern();
        SCHED();
        WAITL(0);
        BAR();
        SCHED();
#pragma unroll
        for (int j = 0; j < 4; ++j) cs[j] = *(LAS f32x4*)(lds + L_CS + ((j >> 1) * 64 + wc * 32 + (j & 1) * 16 + 4 * g) * 4);
        const u32 tg = *(LAS u32*)(lds + L_T);
        mma(X1, W1);
        SCHED();
        T = __builtin_amdgcn_readfirstlane(tg);
      }
      __builtin_amdgcn_s_setprio(2);
      const u32 hvo = OFF_HT + ((rt * 16 + wr * 8) * 54 + ct * 2 + wc) * 1024 + lane * 16;
#pragma unroll
      for (int i = 0; i < 8; ++i) {
        const f32x4 st = *(LAS f32x4*)(lds + L_ST + (wr * 128 + i * 16 + tl) * 16);
        const f32x2 nm = {st.x, st.x}, c1 = {st.y, st.y}, c3 = {st.z, st.z}, vr = {st.w, st.w};
        u32x4 o;
#pragma unroll
        for (int j = 0; j < 2; ++j)
#pragma unroll
          for (int h = 0; h < 2; ++h) {
            f32x2 ag = {acc[i][j][2 * h], acc[i][j][2 * h + 1]}, au = {acc[i][2 + j][2 * h], acc[i][2 + j][2 * h + 1]};
            f32x2 cgv = {cs[j][2 * h], cs[j][2 * h + 1]}, cuv = {cs[2 + j][2 * h], cs[2 + j][2 * h + 1]};
            f32x2 a = __builtin_elementwise_fma(nm, cgv, ag);
            f32x2 bb = __builtin_elementwise_fma(nm, cuv, au);
            f32x2 pv = __builtin_elementwise_fma(a * a, c3, c1) * a;
            f32x2 e = {__builtin_amdgcn_exp2f(pv.x), __builtin_amdgcn_exp2f(pv.y)};
            f32x2 dn = __builtin_elementwise_fma(e, vr, vr);
            f32x2 r = {__builtin_amdgcn_rcpf(dn.x), __builtin_amdgcn_rcpf(dn.y)};
            o[j * 2 + h] = pk2v(a * bb * r);
          }
        st_sc1(rws, o, hvo + i * (54 * 1024));
      }
      SCHED();
      const int ks = ct * 2 + wc, lq = ks < 28 ? ks / 14 : 2 + (ks - 28) / 13, ik = ks - (lq < 2 ? lq * 14 : 2 + lq * 13);
      hfl = lane < 16 ? FL_H + ((rt * 2 + wr) * 16 + lane) * 512 + lq * 128 + ik * 4 : 0u;
    }
    const int t = rq * 64 + wid * 16 + tl;
    u32 oo = t * 1280 + (c2 * 80 + 4 * g) * 2;
    u32x2 xv[5];
    u32x4 stv;
    auto ldx = [&]() {
#pragma unroll
      for (int j = 0; j < 5; ++j) xv[j] = *(const GAS u32x2*)((const char*)x + (size_t)oo + j * 32);
      stv = ld_sc1(rws, t * 16, OFF_ST);
    };
    if (cv) {
      if (wid & 1) conv_rounds<1>(lds, lb, wid, isg ? 1 : 3, chl, hfl, ws, rws, lane, T, ldx);
      else conv_rounds<0>(lds, lb, wid, isg ? 1 : 3, chl, hfl, ws, rws, lane, T, ldx);
    }
    asm volatile("" ::: "memory");
    if (!cv) {
      WAITV(0);
      if (hfl) stflag(ws, hfl, T);
      wait_lds2(lds + L_KF + pr * 8);
      ldx();
      __builtin_amdgcn_s_waitcnt(0x0F70);
    }
    __builtin_amdgcn_s_setprio(0);
    if (b == 0 && wid == 3) {
      u32x4 f0 = {0, 0, 0, 0}, f1 = f0, f2 = f0;
      f0.x = f1.x = f2.x = ~T;
      retry16(f0, ws + FL_ACK, lane * 16, T);
      retry16(f1, ws + FL_ACK + 1024, lane * 16, T);
      retry16(f2, ws + FL_ACK + 2048, (lane < 6 ? lane : 0) * 16, T);
      if (lane == 0) stflag(ws, FL_EPOCH, T);
    }
    retry_st(stv, ws + OFF_ST, t * 16, T);
    f32x4 xs[5], xn[5];
    {
      const float mean = __uint_as_float(stv.x), rstd = __builtin_amdgcn_rsqf(__uint_as_float(stv.z));
#pragma unroll
      for (int j = 0; j < 5; ++j) {
        xs[j] = f32x4{bflo(xv[j].x), bfhi(xv[j].x), bflo(xv[j].y), bfhi(xv[j].y)};
        xn[j] = (xs[j] - mean) * rstd;
      }
    }
    u32 za[5];
#pragma unroll
    for (int j = 0; j < 5; ++j) {
      const int row = j * 16 + 4 * g + q;
      za[j] = L_U + row * 128 + (((wid * 2 + (p >> 1)) ^ (((row >> 1) & 3) << 1)) << 4) + (p & 1) * 8;
      asm volatile("" : "+v"(za[j]), "+v"(xs[j]), "+v"(xn[j]));
    }
    u32 pa[4][5];
#pragma unroll
    for (int w = 0; w < 4; ++w)
#pragma unroll
      for (int j = 0; j < 5; ++j) { const int qq = w * 5 + j, h = (wid - w - 1) & 3; pa[w][j] = (qq < 9 ? 57 + 9 * h + qq : 89 + 11 * h + qq) * 1024 + lane * 16; asm volatile("" : "+v"(pa[w][j])); }
    asm volatile("" : "+v"(oo));
    {
      const int r0 = pr * 40 + (wid & 1) * 24, nr = (wid & 1) ? 16 : 24;
      const u32 zfo = (rq * 8 + c2) * 512 + wid * 128 + (lane < nr ? lane : 0) * 4;
      const u32 zo = (lane >> 3) * 4096 + (((lane & 7) ^ (((lane >> 4) & 3) << 1)) << 4);
      const char* zb = ws + OFF_Z + (c2 * 80 + r0) * 4096 + rq * 128;
      u32 zf;
      wait_lds1(lds + L_P1 + wid * 4);
      aload4_sc1(zf, ws + FL_Z, zfo);
      wait_lds1(lds + L_P2 + wid * 4);
      asm volatile("s_waitcnt vmcnt(0)" : "+v"(zf)::"memory");
      retry4(zf, ws + FL_Z, zfo, T);
      glds_sc1(zb, zo, lb + L_U + r0 * 128);
      glds_sc1(zb + 8 * 4096, zo, lb + L_U + r0 * 128 + 1024);
      if (!(wid & 1)) glds_sc1(zb + 16 * 4096, zo, lb + L_U + r0 * 128 + 2048);
#pragma unroll
      for (int m = 0; m < 3; ++m) {
        wait_cnt(lds + L_P2 + ((wid - m - 1) & 3) * 4, m + 2);
#pragma unroll
        for (int j = 0; j < 5; ++j) xs[j] += *(LAS f32x4*)(lds + pa[m][j]);
#pragma unroll
        for (int j = 0; j < 5; ++j) asm volatile("" : "+v"(xs[j]));
      }
      WAITV(0);
    }
    SCHED();
    BAR();
    SCHED();
#pragma unroll
    for (int j = 0; j < 5; ++j) {
      const f32x4 a3 = *(LAS f32x4*)(lds + pa[3][j]);
      const u32x2 z = trd(lds + za[j]);
      const f32x4 zs = {bflo(z.x), bfhi(z.x), bflo(z.y), bfhi(z.y)};
      const f32x4 a = a3 + (xs[j] + xn[j] * zs);
      u32x2 ov = {pk2(a[0], a[1]), pk2(a[2], a[3])};
      *(GAS u32x2*)((char*)out + oo + j * 32) = ov;
    }
    asm volatile(".Lkf_f:");
  } else {
    const int lw = w4;
    const int ks0 = lw < 2 ? lw * 14 : 28 + (lw - 2) * 13;
    const u32 wso = OFF_WDT + ((c2 * 54 + ks0) * 5) * 1024;
    const u32 a1 = (57 + 9 * lw) * 1024, a2 = (98 + 11 * lw) * 1024, a0 = 12 * lw * 1024;
    auto wdma = [&](int f0, int n, u32 la) {
#pragma unroll
      for (int i = 0; i < n; ++i) glds_sc1(ws + wso + (f0 + i) * 1024, lane * 16, lb + la + i * 1024);
    };
    auto wdmat = [&](int f0, int n, u32 la) {
#pragma unroll
      for (int i = 0; i < n; ++i) { THR(WIN2 - 1); glds_sc1(ws + wso + (f0 + i) * 1024, lane * 16, lb + la + i * 1024); }
    };
    auto kdma = [&]() {
#pragma unroll
      for (int i = 0; i < 2; ++i) glds((const char*)ssk + (size_t)chl * 4096 + ((lw & 1) * 2 + i) * 1024, lane * 16, lb + L_K + pr * KSZ + 64 + ((lw & 1) * 2 + i) * 1024);
    };
    auto udma = [&]() {
#pragma unroll
      for (int i = 0; i < 2; ++i) glds_sc1(ws + OFF_UP + chl * 4096 + ((lw & 1) * 2 + i) * 1024, lane * 16, lb + L_U + pr * 5120 + 512 + ((lw & 1) * 2 + i) * 1024);
    };
    const int nk = lw < 2 ? 14 : 13;
    const u32 fao = ((c2 * 4 + lw) * 8 + xcd) * 128 + (lane < 2 * nk ? lane : 0) * 4;
    u32 dd = 0;
    bf16x8 WF[70];
    auto ldw = [&](int f) { WF[f] = __builtin_bit_cast(bf16x8, ld_sc1(rws, lane * 16 + (f & 3) * 1024, wso + (f & ~3) * 1024)); };
    if (isg) {
      const bool hs = lw * 27 + ct < 64;
      const int srow = rt * 256 + (lw * 27 + ct) * 4 + g;
      u32x4 xr[5];
      const int rid = (3 - lw) * 216 + gidx;
      const bool hr = rid < 540;
      const int rks = rid / 10, rwq = rid % 10;
      const u32 rtl = L_RT + (lw ? 3 - lw : 0) * 4096;
      auto extras = [&]() {
        if (hs) {
#pragma unroll
          for (int i = 0; i < 5; ++i) aload16(xr[i], (const char*)x + i * 256, SROW * 1280 + tl * 16);
        }
        if (hr) {
          const char* wdp = (const char*)wd + (size_t)(rks * 32) * 1280 + rwq * 128;
          const u32 o = (lane >> 3) * 1280 + (((lane & 7) ^ ((lane >> 4) << 1)) << 4);
#pragma unroll
          for (int i = 0; i < 4; ++i) glds(wdp, o + i * 10240, lb + rtl + i * 1024);
        }
      };
      extras();
      T = get_tag(ws);
      if (lw == 0) *(LAS u32*)(lds + L_T) = T;
#define WVE() do { if (!hr) THR(17); else if (!hs) THR(16); else THR(21); } while (0)
      const int f = ((q >> 1) & 1) | ((g & 1) << 1);
      LAS char* wadr = lds + 32768 + (lw >> 1) * 8192 + (8 * g + q) * 128 + ((((lw & 1) * 4 + (p >> 1)) ^ (f << 1)) << 4) + (p & 1) * 8;
      LAS char* wadr1 = lds + 32768 + (lw >> 1) * 8192 + (8 * g + q) * 128 + (((((lw & 1) * 4 + (p >> 1)) ^ (f << 1)) << 4) ^ 32) + (p & 1) * 8;
      u32x2 wv[8];
      f32x4 cm0 = {0.f, 0.f, 0.f, 0.f}, cm1 = cm0;
      const bf16x8 ones = __builtin_bit_cast(bf16x8, u32x4{0x3f803f80u, 0x3f803f80u, 0x3f803f80u, 0x3f803f80u});
      auto csld = [&](int s) {
        LAS char *w0 = wadr + (s % 3) * STG, *w1 = wadr1 + (s % 3) * STG;
        wv[0] = trd(w0); wv[1] = trd(w0 + 512); wv[2] = trd(w0 + 4096); wv[3] = trd(w0 + 4608);
        wv[4] = trd(w1); wv[5] = trd(w1 + 512); wv[6] = trd(w1 + 4096); wv[7] = trd(w1 + 4608);
      };
      auto csum = [&]() {
        cm0 = mfma(ones, cat(wv[0], wv[1]), cm0); cm1 = mfma(ones, cat(wv[4], wv[5]), cm1);
        cm0 = mfma(ones, cat(wv[2], wv[3]), cm0); cm1 = mfma(ones, cat(wv[6], wv[7]), cm1);
        asm volatile("" : "+v"(cm0), "+v"(cm1));
      };
      WVE();
      BAR();
      csld(0); issue(2); csum();
      WVE();
      BAR();
      csld(1); issue(3); csum();
      asm volatile("s_waitcnt vmcnt(24)" : "+v"(xr[0]), "+v"(xr[1]), "+v"(xr[2]), "+v"(xr[3]), "+v"(xr[4])::"memory");
      {
        const rsrc_t rr = mkrsrc(ws, hr ? WS_USED : 0);
        const int row = lane >> 2;
        u32x4 o[4];
#pragma unroll
        for (int dt = 0; dt < 4; ++dt) {
          LAS char* a = lds + rtl + row * 128 + (((dt * 2 + (p >> 1)) ^ (((row >> 1) & 3) << 1)) << 4) + (p & 1) * 8;
          u32x2 lo = trd(a), hi = trd(a + 2048);
          o[dt] = u32x4{lo.x, lo.y, hi.x, hi.y};
        }
#pragma unroll
        for (int dt = 0; dt < 4; ++dt) { const int Dg = rwq * 4 + dt; st_sc1(rr, o[dt], OFF_WDT + (((Dg / 5) * 54 + rks) * 5 + Dg % 5) * 1024 + lane * 16); }
      }
      SCHED();
      WAITV(16);
      BAR();
      csld(2); issue(4); csum();
      float mean; f32x2 s2;
      SCHED();
      row_stats1(xr, mean, s2);
      SCHED();
      WAITV(16);
      BAR();
      csld(3); issue(5); csum();
      SCHED();
      row_stats2(xr, mean, s2, mkrsrc(ws, hs ? WS_USED : 0), srow, tl, T);
      SCHED();
      WAITV(13);
      BAR();
      csld(4); issue(6);
      {
        const int m = rwq % 5, lq = rks < 28 ? rks / 14 : 2 + (rks - 28) / 13, ik = rks - (lq < 2 ? lq * 14 : 2 + lq * 13);
        u32 rfo = FL_A + (((rwq / 5) * 4 + m) * 4 + lq) * 1024 + ik * 8, rfm = (m < 4 ? 0xffu : 0u) | (m > 0 ? 0xff00u : 0u);
        if (!hr) { rfo = FL_ACK + gidx * 4; rfm = 1u; }
        if (lane < 16 && ((rfm >> lane) & 1)) stflag(ws, rfo + (lane & 7) * 128 - (lane >> 3) * 4092, T);
      }
      csum();
      WAITV(14);
      BAR();
      csld(5); issue(7); csum();
      WAITV(13);
      BAR();
      csld(6); issue(8); csum();
      WAITV(12);
      BAR();
      csld(7);
      u32x4 st;
      u32 fa, fu;
      const u32 fuo = ((chl >> 6) * 16 + (gidx & 15)) * 128 + (lane & 31) * 4;
      aload4_sc1(fa, ws + FL_A, fao);
      aload16_sc1(st, ws + OFF_ST + (rt * 256 + lw * 64) * 16, lane * 16);
      issue(9);
      csum();
      asm volatile("s_waitcnt vmcnt(12)" : "+v"(fa), "+v"(st)::"memory");
      BAR();
      csld(8);
      asm volatile("" : "+s"(ssD));
      dd = (*(const CAS u32*)(uintptr_t)(ssD + (chl & ~1)) >> ((chl & 1) * 16)) & 0xffffu;
      aload4_sc1(fu, ws + FL_U, fuo);
      kdma();
      if (!(lw & 1)) conv_pads(lds + L_K + pr * KSZ, lds + L_U + pr * 5120, lane);
      retry4(fa, ws + FL_A, fao, T);
      wdma(32, 9, a1);
      SCHED();
#pragma unroll
      for (int f = 32 - NP8; f < 32; ++f) ldw(f);
      SCHED();
      csum();
      asm volatile("s_waitcnt vmcnt(%1)" : "+v"(fu) : "n"(11 + NP8) : "memory");
      BAR();
      csld(9);
      retry_st(st, ws + OFF_ST + (rt * 256 + lw * 64) * 16, lane * 16, T);
      {
        const float K1 = -2.0f * 0.7978845608028654f * 1.4426950408889634f, K2 = K1 * 0.044715f;
        const float v = __uint_as_float(st.z), rs = __builtin_amdgcn_rsqf(v);
        *(LAS f32x4*)(lds + L_ST + (lw * 64 + lane) * 16) = f32x4{-__uint_as_float(st.x), K1 * rs, K2 * rs * rs * rs, v};
      }
      csum();
      if (lane < 16) { *(LAS float*)(lds + L_CS + (lw * 32 + lane) * 4) = cm0[0]; *(LAS float*)(lds + L_CS + (lw * 32 + 16 + lane) * 4) = cm1[0]; }
      wdma(41, A2PRE, a2);
      WAITL(0);
      BAR();
      retry4(fu, ws + FL_U, fuo, T);
      udma();
      wdma(41 + A2PRE, 11 - A2PRE, a2 + A2PRE * 1024);
    } else {
      wait_flags<8>(ws, FL_A + fao, T);
      wait_lds4(lds + L_SD);
      wait_lds4(lds + L_SD + 16);
      wdma(32, 9, a1);
      wdma(41, 11, a2);
      SCHED();
#pragma unroll
      for (int f = 32 - NP8; f < 32; ++f) ldw(f);
      SCHED();
    }
#pragma unroll
    for (int f = 0; f < 12; ++f) { THR(WIN - 1); SCHED(); WF[f] = __builtin_bit_cast(bf16x8, ld_sc1(rws, lane * 16 + (f & 3) * 1024, wso + (f & ~3) * 1024)); SCHED(); }
    SCHED();
    if (isg) {
      asm volatile("s_waitcnt vmcnt(%0)" ::"n"(12 + 11 - A2PRE) : "memory");
      if (!(lw & 1)) conv_patch(lds + L_K + pr * KSZ, dd, lane);
      WAITL(0);
      set_lds(lds + L_KF + lw * 4, lane);
      __builtin_amdgcn_s_setprio(0);
    }
    SCHED();
#pragma unroll
    for (int f = 12; f < 32 - NP8; ++f) { THR((f < WSW ? WIN : WIN2) - 1); SCHED(); ldw(f); SCHED(); }
    SCHED();
    if (isg) {
      wdmat(52, 12, a0);
    } else {
      u32x4 R[12];
#pragma unroll
      for (int i = 0; i < 12; ++i) R[i] = ld_sc1(rws, lane * 16 + (i & 3) * 1024, wso + (52 + (i & ~3)) * 1024);
      SCHED();
      __builtin_amdgcn_s_waitcnt(0x0F70);
      wait_lds4(lds + L_CF);
#pragma unroll
      for (int i = 0; i < 12; ++i) *(LAS u32x4*)(lds + a0 + i * 1024 + lane * 16) = R[i];
    }
    wait_flags<1>(ws, FL_H + (rq * 8 + c2) * 512 + lw * 128 + (lane < nk ? lane : 0) * 4, T);
    const u32 hso = OFF_HT + (rq * 4 * 54 + ks0) * 1024;
    f32x4 acc[4][5];
    {
      bf16x8 H[14][4];
      const rsrc_t rl = mkrsrc(ws, lw < 2 ? WS_USED : 0);
      auto ldh1 = [&](int k, int i) { H[k][i] = __builtin_bit_cast(bf16x8, ld_sc1(k == 13 ? rl : rws, lane * 16 + (k & 3) * 1024, hso + ((i + lw + 1) & 3) * (54 * 1024) + (k & ~3) * 1024)); };
      auto ldh = [&](int k) {
#pragma unroll
        for (int i = 0; i < 4; ++i) ldh1(k, i);
      };
      auto ldt = [&](int n0) {
#pragma unroll
        for (int n = n0; n < n0 + 4; ++n) ldh1(11 + n % 3, n / 3);
      };
      auto ldl = [&](int f) {
        if (f >= 64) return;
        const u32 o = f < 41 ? a1 + (f - 32) * 1024 : f < 52 ? a2 + (f - 41) * 1024 : a0 + (f - 52) * 1024;
        WF[f] = *(LAS bf16x8*)(lds + o + lane * 16);
      };
      ldh(0); ldh(1); ldh(2);
      SCHED();
#pragma unroll
      for (int k = 0; k < 11; ++k) {
#pragma unroll
        for (int j = 0; j < 5; ++j) {
          const int f = k * 5 + j;
#pragma unroll
          for (int i = 0; i < 4; ++i) acc[i][j] = mfma(WF[f], H[k][i], k == 0 ? f32x4{0.f, 0.f, 0.f, 0.f} : acc[i][j]);
          if (f < 44) { if (f + 4 >= 32) ldl(f + 4); }
          else { ldl(48 + (f - 44) * 2); ldl(49 + (f - 44) * 2); }
          SCHED();
        }
        if (k == 0) ldh(3);
        else if (k == 1) { ldh(4); ldh(5); }
        else if (k < 7) ldh(k + 4);
        else if (k < 10) ldt((k - 7) * 4);
        if (k == 7) {
#pragma unroll
          for (int f = 64; f < 70; ++f) WF[f] = __builtin_bit_cast(bf16x8, ld_sc1(f == 64 ? rws : rl, lane * 16 + (f & 3) * 1024, wso + (f & ~3) * 1024));
        }
        if (k == ZK) *(volatile LAS u32*)(lds + L_P1 + lw * 4) = 1u;
        if (k == ZK2) *(volatile LAS u32*)(lds + L_P2 + lw * 4) = 1u;
        SCHED();
      }
      auto wrp = [&](int i, int j) { const int qq = i * 5 + j; *(LAS f32x4*)(lds + (qq < 9 ? a1 + qq * 1024 : a2 + (qq - 9) * 1024) + lane * 16) = acc[i][j]; };
#pragma unroll
      for (int i = 0; i < 4; ++i) {
#pragma unroll
        for (int n = 0; n < 15; ++n) {
          acc[i][n % 5] = mfma(WF[55 + n], H[11 + n / 5][i], acc[i][n % 5]);
          SCHED();
          if (n >= 12) wrp(i, n - 12);
          if (i > 0 && n < 2 * SP && n % SP == SP - 1) {
            wrp(i - 1, 3 + n / SP);
            if (n / SP == 1) { asm volatile("" ::: "memory"); *(volatile LAS u32*)(lds + L_P2 + lw * 4) = i + 1; }
          }
          SCHED();
        }
      }
      wrp(3, 3); wrp(3, 4);
    }
    WAITL(0);
    BAR();
    asm volatile(".Lkf_e:");
  }
}

extern "C" void kernel_launch(void* const* d_in, const int* in_sizes, int n_in, void* d_out, int out_size, void* d_ws, size_t ws_size,
                              hipStream_t stream) {
  kf<<<256, 512, 0, stream>>>((const u16*)d_in[0], (const u16*)d_in[1], (const u16*)d_in[2], (const u16*)d_in[3], (const u16*)d_in[4],
                              (const u16*)d_in[5], (const u16*)d_in[7], (const u16*)d_in[8], (const u16*)d_in[9], (char*)d_ws, (u16*)d_out);
}
```
